```python
import torch

def _softmax(x, axis=-1):
    return torch.softmax(x, dim=axis)
def _gelu(x):
    return torch.nn.functional.gelu(x, approximate="tanh")
def _silu(x):
    return torch.nn.functional.silu(x)
def _sigmoid(x):
    return torch.sigmoid(x)
def _layernorm(x, axis=-1, eps=1e-5):
    m = x.mean(dim=axis, keepdim=True)
    v = x.var(dim=axis, keepdim=True, unbiased=False)
    return (x - m) / torch.sqrt(v + eps)
def _scatter_add(tgt, idx, src, axis=0):
    return tgt.clone().index_add_(axis, idx, src)
def _segment_sum(src, seg, n):
    out = torch.zeros((n,) + src.shape[1:], dtype=src.dtype)
    return out.index_add_(0, seg, src)
def _pad(x, pw):
    flat = tuple(v for pair in reversed(pw) for v in pair)
    return torch.nn.functional.pad(x, flat)

kernel_name = 'standalone__mha_block_cached__near_ridge'

def setup_inputs():

    _w = (lambda a: (lambda t: t.to(torch.bfloat16) if t.is_floating_point() else t)(torch.as_tensor(a)))
    B, Sq, D = 16, 96, 640
    H, Sk, Dh = 32, 512, 128
    return {
        "x": _w((lambda *s: torch.randn(*s, dtype=torch.bfloat16) * 0.8)(B, Sq, D)),
        "k_cache": _w((lambda *s: torch.randn(*s, dtype=torch.bfloat16) * 0.8)(B, H, Sk, Dh)),
        "v_cache": _w((lambda *s: torch.randn(*s, dtype=torch.bfloat16) * 0.05)(B, H, Sk, Dh)),
        "wq": _w((lambda *s: torch.randn(*s, dtype=torch.bfloat16) * 0.8)(D, H * Dh) * (D ** -0.5)),
        "wo": _w((lambda *s: torch.randn(*s, dtype=torch.bfloat16) * 0.05)(H * Dh, D) * ((H * Dh) ** -0.5)),
    }

def reference(x, k_cache, v_cache, wq, wo):
    B, Sq, D = x.shape
    H, Dh = 32, 128
    q = (lambda x, *d: x.permute(*d))((x @ wq).reshape(B, Sq, H, Dh), 0, 2, 1, 3)
    scores = torch.einsum("bhqd,bhkd->bhqk", q, k_cache) * (Dh ** -0.5)
    p = _softmax(scores, axis=-1)
    o = torch.einsum("bhqk,bhkd->bhqd", p, v_cache)
    return (lambda x, *d: x.permute(*d))(o, 0, 2, 1, 3).reshape(B, Sq, H * Dh) @ wo
```

```cpp
#include <hip/hip_runtime.h>
#include <hip/hip_bf16.h>

typedef unsigned short u16;
using bf16x8 = __attribute__((ext_vector_type(8))) short;
using f32x4 = __attribute__((ext_vector_type(4))) float;

constexpr int B = 16, SQ = 96, D = 640, H = 32, SK = 512, DH = 128;
constexpr int M = B * SQ, HD = H * DH;

__device__ __forceinline__ u16 f2bf(float x) { __hip_bfloat16 b = __float2bfloat16(x); return *reinterpret_cast<u16*>(&b); }

template <int N, int K>
__global__ __launch_bounds__(256) void gemm_simple(const u16* __restrict__ A, const u16* __restrict__ Bm, u16* __restrict__ C) {
  const int lane = threadIdx.x & 63, w = threadIdx.x >> 6;
  const int m0 = blockIdx.y * 64 + (w >> 1) * 32, n0 = blockIdx.x * 64 + (w & 1) * 32;
  const int r = lane & 15, g = lane >> 4;
  f32x4 acc[2][2] = {};
  for (int k0 = 0; k0 < K; k0 += 32) {
    bf16x8 a[2], b[2];
    for (int i = 0; i < 2; ++i) a[i] = *reinterpret_cast<const bf16x8*>(A + (size_t)(m0 + 16 * i + r) * K + k0 + 8 * g);
    for (int i = 0; i < 2; ++i)
      for (int j = 0; j < 8; ++j) b[i][j] = (short)Bm[(size_t)(k0 + 8 * g + j) * N + n0 + 16 * i + r];
    for (int i = 0; i < 2; ++i)
      for (int j = 0; j < 2; ++j) acc[i][j] = __builtin_amdgcn_mfma_f32_16x16x32_bf16(a[i], b[j], acc[i][j], 0, 0, 0);
  }
  for (int i = 0; i < 2; ++i)
    for (int j = 0; j < 2; ++j)
      for (int q = 0; q < 4; ++q) C[(size_t)(m0 + 16 * i + 4 * g + q) * N + n0 + 16 * j + r] = f2bf(acc[i][j][q]);
}

__global__ __launch_bounds__(384) void attn_simple(const u16* __restrict__ Q, const u16* __restrict__ Kc, const u16* __restrict__ Vc, u16* __restrict__ O) {
  __shared__ __attribute__((aligned(16))) u16 P[6][16][SK + 8];
  const int lane = threadIdx.x & 63, w = threadIdx.x >> 6;
  const int b = blockIdx.x / H, h = blockIdx.x % H;
  const int r = lane & 15, g = lane >> 4;
  const u16* q = Q + (size_t)(b * SQ + w * 16) * HD + h * DH;
  const u16* k = Kc + (size_t)(b * H + h) * SK * DH;
  const u16* v = Vc + (size_t)(b * H + h) * SK * DH;
  bf16x8 qa[4];
  for (int s = 0; s < 4; ++s) qa[s] = *reinterpret_cast<const bf16x8*>(q + (size_t)r * HD + 32 * s + 8 * g);
  f32x4 S[32];
#pragma unroll
  for (int t = 0; t < 32; ++t) {
    f32x4 acc = {};
    for (int s = 0; s < 4; ++s) {
      bf16x8 kb = *reinterpret_cast<const bf16x8*>(k + (size_t)(16 * t + r) * DH + 32 * s + 8 * g);
      acc = __builtin_amdgcn_mfma_f32_16x16x32_bf16(qa[s], kb, acc, 0, 0, 0);
    }
    S[t] = acc;
  }
  const float sc = 0.088388347648318447f * 1.4426950408889634f;
  float inv[4];
#pragma unroll
  for (int i = 0; i < 4; ++i) {
    float m = -INFINITY;
#pragma unroll
    for (int t = 0; t < 32; ++t) m = fmaxf(m, S[t][i]);
    for (int o = 1; o < 16; o <<= 1) m = fmaxf(m, __shfl_xor(m, o));
    float l = 0.f;
#pragma unroll
    for (int t = 0; t < 32; ++t) { float p = exp2f((S[t][i] - m) * sc); l += p; P[w][4 * g + i][16 * t + r] = f2bf(p); }
    for (int o = 1; o < 16; o <<= 1) l += __shfl_xor(l, o);
    inv[i] = 1.f / l;
  }
  __syncthreads();
  f32x4 o[8] = {};
  for (int s = 0; s < 16; ++s) {
    bf16x8 pa = *reinterpret_cast<const bf16x8*>(&P[w][r][32 * s + 8 * g]);
#pragma unroll
    for (int c = 0; c < 8; ++c) {
      bf16x8 vb;
      for (int j = 0; j < 8; ++j) vb[j] = (short)v[(size_t)(32 * s + 8 * g + j) * DH + 16 * c + r];
      o[c] = __builtin_amdgcn_mfma_f32_16x16x32_bf16(pa, vb, o[c], 0, 0, 0);
    }
  }
  u16* op = O + (size_t)(b * SQ + w * 16) * HD + h * DH;
#pragma unroll
  for (int c = 0; c < 8; ++c)
    for (int i = 0; i < 4; ++i) op[(size_t)(4 * g + i) * HD + 16 * c + r] = f2bf(o[c][i] * inv[i]);
}

extern "C" void kernel_launch(void* const* d_in, const int* in_sizes, int n_in,
                              void* d_out, int out_size, void* d_ws, size_t ws_size,
                              hipStream_t stream) {
  const u16* x = (const u16*)d_in[0];
  const u16* kc = (const u16*)d_in[1];
  const u16* vc = (const u16*)d_in[2];
  const u16* wq = (const u16*)d_in[3];
  const u16* wo = (const u16*)d_in[4];
  u16* q = (u16*)d_ws;
  u16* o = q + (size_t)M * HD;
  gemm_simple<HD, D><<<dim3(HD / 64, M / 64), 256, 0, stream>>>(x, wq, q);
  attn_simple<<<B * H, 384, 0, stream>>>(q, kc, vc, o);
  gemm_simple<D, HD><<<dim3(D / 64, M / 64), 256, 0, stream>>>(o, wo, (u16*)d_out);
}
```

```cpp
#include <hip/hip_runtime.h>

typedef short bf16x8 __attribute__((ext_vector_type(8)));
typedef short s16x4 __attribute__((ext_vector_type(4)));
typedef float f32x16 __attribute__((ext_vector_type(16)));
typedef float f32x4 __attribute__((ext_vector_type(4)));
typedef float f32x2 __attribute__((ext_vector_type(2)));
typedef unsigned u32x4 __attribute__((ext_vector_type(4)));
typedef unsigned u32x2 __attribute__((ext_vector_type(2)));
typedef __bf16 bf16x2n __attribute__((ext_vector_type(2)));
#define LAS __attribute__((address_space(3)))
typedef LAS const char* lds_cptr;
typedef volatile LAS unsigned* lds_flag;

constexpr int B = 16, SQ = 96, D = 640, H = 32, SK = 512, DH = 128, HD = H * DH;
constexpr int SLOT = 16384;
constexpr int WSLOT = 45056;
constexpr int LDS_Q0 = 0, LDS_QB = 131072  , LDS_FLAG = 155648, LDS_BYTES = LDS_FLAG + 256;
constexpr int T_W0 = 65536, T_W1 = 0, T_O1 = 106496, T_OA = 40960;
constexpr int F_KVL = 0, F_KVP = 4, F_EP = 8, F_OC = 12, F_OL = 16;
constexpr unsigned WS_P = 16u << 20, WS_P_BYTES = 16 * 4 * 4 * 15 * 4096, WS_F = 40u << 20;
constexpr unsigned FO_EPOCH = 0, FO_SEEN = 64, FO_O = FO_SEEN + 256 * 16, FO_P = FO_O + 512 * 32;
constexpr float CS = 0.088388347648318447f * 1.4426950408889634f;
constexpr int PRE_PARTS = 3;
constexpr float THR = 8.0f;

#define STAMP(k)

#define MFMA32(a, b, c) __builtin_amdgcn_mfma_f32_32x32x16_bf16(a, b, c, 0, 0, 0)
#define SBAR() __builtin_amdgcn_sched_barrier(0)
#define MEMBAR() asm volatile("" ::: "memory")
#define PIN(x) asm volatile("" : "+v"(x))
#define DI __device__ __forceinline__
#define WAIT_VM(n) asm volatile("s_waitcnt vmcnt(" #n ")" ::: "memory")
#define WAIT_LGKM0() asm volatile("s_waitcnt lgkmcnt(0)" ::: "memory")
#define RAWBAR() do { WAIT_LGKM0(); __builtin_amdgcn_s_barrier(); MEMBAR(); } while (0)

DI unsigned cvtpk(float lo, float hi) { f32x2 v = {lo, hi}; return __builtin_bit_cast(unsigned, __builtin_convertvector(v, bf16x2n)); }
DI s16x4 trd(lds_cptr p) { return __builtin_amdgcn_ds_read_tr16_b64_v4i16((LAS s16x4*)p); }
DI bf16x8 cat(s16x4 a, s16x4 b) { return __builtin_shufflevector(a, b, 0, 1, 2, 3, 4, 5, 6, 7); }
template <bool SCALE> DI bf16x8 pack8(const f32x16& x, int s) {
  const float c = SCALE ? CS : 1.0f;
  u32x4 w = {cvtpk(x[8 * s] * c, x[8 * s + 1] * c), cvtpk(x[8 * s + 2] * c, x[8 * s + 3] * c), cvtpk(x[8 * s + 4] * c, x[8 * s + 5] * c), cvtpk(x[8 * s + 6] * c, x[8 * s + 7] * c)};
  return __builtin_bit_cast(bf16x8, w);
}
DI unsigned rfl(unsigned v) { return __builtin_amdgcn_readfirstlane(v); }
DI unsigned lds_addr(lds_flag f) { return (unsigned)(uintptr_t)f; }

DI void wait1(unsigned early, lds_flag f, unsigned need) {
  unsigned t, sv, cnt;
  asm volatile("s_cmp_ge_u32 %3, %4\n\ts_cbranch_scc1 W1E%=\n\ts_mov_b32 %2, 0\n"
               "W1L%=:\n\tds_read_b32 %0, %5\n\ts_waitcnt lgkmcnt(0)\n\tv_readfirstlane_b32 %1, %0\n\ts_cmp_ge_u32 %1, %4\n\ts_cbranch_scc1 W1E%=\n\t"
               "s_sleep 1\n\ts_add_u32 %2, %2, 1\n\ts_cmp_lt_u32 %2, 0x4000\n\ts_cbranch_scc1 W1L%=\n"
               "W1E%=:"
               : "=&v"(t), "=&s"(sv), "=&s"(cnt) : "s"(early), "s"(need), "v"(lds_addr(f)) : "memory", "scc");
}
DI void wait4(lds_flag f, unsigned need) {
  unsigned a0, a1, a2, m, sv, cnt;
  asm volatile("s_mov_b32 %5, 0\n"
               "W4L%=:\n\tds_read_b32 %3, %7\n\tds_read_b32 %0, %7 offset:4\n\tds_read_b32 %1, %7 offset:8\n\tds_read_b32 %2, %7 offset:12\n\ts_waitcnt lgkmcnt(0)\n\t"
               "v_min_u32 %3, %3, %0\n\tv_min3_u32 %3, %3, %1, %2\n\ts_nop 0\n\tv_readfirstlane_b32 %4, %3\n\ts_cmp_ge_u32 %4, %6\n\ts_cbranch_scc1 W4E%=\n\t"
               "s_sleep 1\n\ts_add_u32 %5, %5, 1\n\ts_cmp_lt_u32 %5, 0x4000\n\ts_cbranch_scc1 W4L%=\n"
               "W4E%=:"
               : "=&v"(a0), "=&v"(a1), "=&v"(a2), "=&v"(m), "=&s"(sv), "=&s"(cnt) : "s"(need), "v"(lds_addr(f)) : "memory", "scc");
}
DI unsigned min4(lds_flag f) { u32x4 v = *(volatile LAS u32x4*)f; unsigned m = v[0] < v[1] ? v[0] : v[1]; m = m < v[2] ? m : v[2]; return rfl(m < v[3] ? m : v[3]); }
DI unsigned lds_add_ret(lds_flag f, int lane) {
  unsigned old = 0;
  if (lane == 0) old = __hip_atomic_fetch_add((LAS unsigned*)f, 1u, __ATOMIC_RELAXED, __HIP_MEMORY_SCOPE_WORKGROUP);
  return rfl(old);
}
DI unsigned gld(const unsigned* p) { return __hip_atomic_load(p, __ATOMIC_RELAXED, __HIP_MEMORY_SCOPE_AGENT); }
DI void gst(unsigned* p, unsigned v) { __hip_atomic_store(p, v, __ATOMIC_RELAXED, __HIP_MEMORY_SCOPE_AGENT); }
DI void gwait(const unsigned* p, unsigned tag) {
  for (unsigned spins = 0; spins < 2048u; ++spins) {
    if (__all(gld(p) == tag)) break;
    __builtin_amdgcn_s_sleep(2);
  }
  MEMBAR();
}

#define DMA_HEAD "s_nop 2\n\ts_mov_b32 m0, %0\n\ts_nop 0\n\t"
#define DMA_BODY(POL) \
  if constexpr (N == 1) asm volatile(DMA_HEAD "buffer_load_dwordx4 %1, %2, %3 offen" POL " lds" ::"s"(dst), "v"(va), "s"(srd), "s"(soff) : "m0", "memory"); \
  else if constexpr (N == 2) asm volatile(DMA_HEAD "buffer_load_dwordx4 %1, %3, %4 offen" POL " lds\n\tbuffer_load_dwordx4 %2, %3, %4 offen offset:1024" POL " lds" \
                                          ::"s"(dst), "v"(va), "v"(vb), "s"(srd), "s"(soff) : "m0", "memory"); \
  else if constexpr (N == 3) asm volatile(DMA_HEAD "buffer_load_dwordx4 %1, %4, %5 offen" POL " lds\n\tbuffer_load_dwordx4 %2, %4, %5 offen offset:1024" POL " lds\n\t" \
                                          "buffer_load_dwordx4 %3, %4, %5 offen offset:2048" POL " lds" ::"s"(dst), "v"(va), "v"(vb), "v"(vc), "s"(srd), "s"(soff) : "m0", "memory"); \
  else asm volatile(DMA_HEAD "buffer_load_dwordx4 %1, %5, %6 offen" POL " lds\n\tbuffer_load_dwordx4 %2, %5, %6 offen offset:1024" POL " lds\n\t" \
                    "buffer_load_dwordx4 %3, %5, %6 offen offset:2048" POL " lds\n\tbuffer_load_dwordx4 %4, %5, %6 offen offset:3072" POL " lds" \
                    ::"s"(dst), "v"(va), "v"(vb), "v"(vc), "v"(vd), "s"(srd), "s"(soff) : "m0", "memory");
template <int N, int POL>
DI void dma(__amdgpu_buffer_rsrc_t srd, unsigned soff, unsigned dst, unsigned va, unsigned vb = 0, unsigned vc = 0, unsigned vd = 0) {
  dst = rfl(dst); soff = rfl(soff);
  if constexpr (POL == 0) { DMA_BODY("") } else if constexpr (POL == 1) { DMA_BODY(" nt") } else { DMA_BODY(" sc1") }
}
template <int PITCH> DI unsigned dma_voff(int i) { return (i >> 4) * PITCH + 16 * ((i & 15) ^ ((i >> 4) << 2)); }

DI int wslot(int t) { return (t % 3) * WSLOT; }
DI void qproj(lds_cptr lds, int i, int lane, f32x16 (&acc)[2][3]) {
  const int hi = lane >> 5, blk = (lane >> 4) & 1, q = (lane & 15) >> 2, pp = lane & 3, r32 = lane & 31;
  lds_cptr aw[4], ax[4];
#pragma unroll
  for (int d = 0; d < 2; ++d)
#pragma unroll
    for (int t = 0; t < 2; ++t)
      aw[2 * d + t] = lds + 16384 * (i >> 1) + 2048 * hi + 1024 * t + 256 * q + 16 * (4 * ((2 * (i & 1) + d) ^ q) + ((2 * blk + (pp & 1)) ^ (2 * hi + t))) + 8 * (pp >> 1);
#pragma unroll
  for (int u = 0; u < 4; ++u) ax[u] = lds + 32768 + 128 * r32 + 16 * ((2 * u + hi) ^ ((r32 >> 1) & 7));
  bf16x8 wf[2][2], xf[2][3];
  auto ldw = [&](int buf, int so, int u, int d) { wf[buf][d] = cat(trd(aw[2 * d] + so + 4096 * u), trd(aw[2 * d + 1] + so + 4096 * u)); };
  auto ldx = [&](int buf, int so, int u, int qb) { xf[buf][qb] = *(const LAS bf16x8*)(ax[u] + so + 4096 * qb); };
  RAWBAR();
  int so = wslot(0);
#pragma unroll
  for (int d = 0; d < 2; ++d) ldw(0, so, 0, d);
#pragma unroll
  for (int qb = 0; qb < 3; ++qb) ldx(0, so, 0, qb);
#pragma unroll 1
  for (int t = 0; t < 10; ++t) {
    const int son = wslot(t + 1);
#pragma unroll
    for (int u = 0; u < 4; ++u) {
      SBAR();
      const int nb = (u + 1) & 1, nso = u < 3 ? so : son, nu = (u + 1) & 3;
#pragma unroll
      for (int g = 0; g < 6; ++g) {
        const int d = g / 3, qb = g % 3;
        acc[d][qb] = MFMA32(wf[u & 1][d], xf[u & 1][qb], acc[d][qb]);
        if (u < 3) {
          if (g == 0) ldw(nb, nso, nu, 0);
          else if (g < 4) ldx(nb, nso, nu, g - 1);
          else if (g == 4) ldw(nb, nso, nu, 1);
        } else {
          if (g == 2) { SBAR(); RAWBAR(); ldw(nb, nso, nu, 0); ldx(nb, nso, nu, 0); }
          else if (g == 3) { ldx(nb, nso, nu, 1); ldx(nb, nso, nu, 2); }
          else if (g == 4) ldw(nb, nso, nu, 1);
        }
        SBAR();
      }
    }
    so = son;
  }
}

struct AAddr { lds_cptr k[8]; lds_cptr v[8]; };
DI void aaddr(AAddr& a, lds_cptr kv, int lane) {
  const int r32 = lane & 31, hi = lane >> 5, blk = (lane >> 4) & 1, q = (lane & 15) >> 2, p = lane & 3;
  const int f = ((r32 & 3) << 2) | ((r32 >> 2) & 3);
#pragma unroll
  for (int s = 0; s < 8; ++s) a.k[s] = kv + 256 * r32 + 16 * ((2 * s + hi) ^ f);
#pragma unroll
  for (int c = 0; c < 4; ++c)
#pragma unroll
    for (int t = 0; t < 2; ++t)
      a.v[2 * c + t] = kv + 2 * SLOT + 2048 * t + 1024 * hi + 256 * q + 16 * (4 * (c ^ q) + ((2 * blk + (p >> 1)) ^ (2 * t + hi))) + 8 * (p & 1);
}
DI float max3(float a, float b, float c) { return __builtin_fmaxf(__builtin_fmaxf(a, b), c); }
DI void attn_head(AAddr& a, lds_flag fl, int n0  , lds_flag prog, int lane, bf16x8 (&qf)[8], lds_cptr qnext,
                  __amdgpu_buffer_rsrc_t so, unsigned ooff, unsigned* pend_flag, unsigned tag, LAS char* olds  , unsigned olds_need, lds_flag olds_cnt) {
  f32x16 o[4] = {};
  f32x16 ol = {};
  f32x16 negr = {};
  const bf16x8 ones = {0x3F80, 0x3F80, 0x3F80, 0x3F80, 0x3F80, 0x3F80, 0x3F80, 0x3F80};
  bf16x8 fr[2][4];
  auto ldk = [&](int step, int i) {
    const int tt = step >> 2, kb = (step >> 1) & 1, hf = step & 1;
    fr[step & 1][i] = *(const LAS bf16x8*)(a.k[4 * hf + i] + SLOT * tt + 8192 * kb);
  };
  auto ldv = [&](int step, int c) {
    const int tt = step >> 2, ks = step & 3;
    fr[step & 1][c] = cat(trd(a.v[2 * c] + SLOT * tt + 4096 * ks), trd(a.v[2 * c + 1] + SLOT * tt + 4096 * ks));
  };
  wait1(0, fl + F_KVL, n0 + 1);
#pragma unroll
  for (int i = 0; i < 4; ++i) ldk(0, i);
#pragma unroll 1
  for (int ch = 0; ch < 4; ++ch) {
    const int n = n0 + 8 * ch;
    f32x16 s[4];
    unsigned early = 0;
    float mx = -__builtin_inff();
#pragma unroll
    for (int step = 0; step < 8; ++step) {
      if (step & 1) { MEMBAR(); prog[0] = n + (step >> 1) + 1; if (step < 7) wait1(rfl(early), fl + F_KVL, n + (step >> 1) + 2); }
      SBAR();
      const int ti = step >> 1, hf = step & 1;
#pragma unroll
      for (int i = 0; i < 4; ++i) {
        if (hf == 0 && i == 0) s[ti] = MFMA32(fr[step & 1][0], qf[0], negr);
        else s[ti] = MFMA32(fr[step & 1][i], qf[4 * hf + i], s[ti]);
        if (step + 1 < 8) ldk(step + 1, i);
        if ((!(step & 1) || step == 7) && i == 0) early = fl[F_KVL];
        if (ti >= 1 && (hf == 1 || i >= 2)) {
          const int g = hf == 0 ? i - 2 : 2 + i;
          const f32x16& x = s[ti - 1];
          if (g < 2) { mx = max3(mx, x[4 * g], x[4 * g + 1]); mx = max3(mx, x[4 * g + 2], x[4 * g + 3]); }
          else mx = max3(mx, x[8 + 2 * (g - 2)], x[9 + 2 * (g - 2)]);
          PIN(mx);
        }
        SBAR();
      }
    }
#pragma unroll
    for (int i = 0; i < 8; ++i) a.k[i] += (ch & 1) ? 65536 : -65536;
    wait1(rfl(early), fl + F_KVL, n + 5);
#pragma unroll
    for (int c = 0; c < 4; ++c) ldv(0, c);
    SBAR();
#pragma unroll
    for (int r = 0; r < 16; r += 2) mx = max3(mx, s[3][r], s[3][r + 1]);
    { auto rr = __builtin_amdgcn_permlane32_swap(__float_as_uint(mx), __float_as_uint(mx), false, false);
      mx = __builtin_fmaxf(__uint_as_float(rr[0]), __uint_as_float(rr[1])); }
    if (ch == 0 || __any(mx > THR)) {
      const float dl = ch == 0 ? mx : __builtin_fmaxf(mx, 0.f);
#pragma unroll
      for (int i = 0; i < 4; ++i)
#pragma unroll
        for (int r = 0; r < 16; ++r) s[i][r] -= dl;
#pragma unroll
      for (int r = 0; r < 16; ++r) negr[r] -= dl;
      if (ch > 0) {
        const float alpha = __builtin_amdgcn_exp2f(-dl);
        ol[0] *= alpha;
#pragma unroll
        for (int c = 0; c < 4; ++c)
#pragma unroll
          for (int r = 0; r < 16; ++r) o[c][r] *= alpha;
      }
    }
    unsigned pw[3][4];
    float pe[2][2];
    auto expo = [&](int step, int i) {
      const f32x16& x = s[step >> 1];
      const int e = 8 * (step & 1) + 2 * i;
      pe[i & 1][0] = __builtin_amdgcn_exp2f(x[e]); pe[i & 1][1] = __builtin_amdgcn_exp2f(x[e + 1]);
      PIN(pe[i & 1][0]); PIN(pe[i & 1][1]);
    };
    auto packw = [&](int step, int i) { pw[step % 3][i] = cvtpk(pe[i & 1][0], pe[i & 1][1]); PIN(pw[step % 3][i]); };
#pragma unroll
    for (int st = 0; st < 2; ++st)
#pragma unroll
      for (int i = 0; i < 4; ++i) { expo(st, i); packw(st, i); }
    SBAR();
#pragma unroll
    for (int step = 0; step < 8; ++step) {
      if (step & 1) { MEMBAR(); prog[0] = n + 4 + (step >> 1) + 1; wait1(rfl(early), fl + F_KVL, (step < 7 || ch < 3) ? n + 4 + (step >> 1) + 2 : 0); }
      SBAR();
      u32x4 pv = {pw[step % 3][0], pw[step % 3][1], pw[step % 3][2], pw[step % 3][3]};
      const bf16x8 pf = __builtin_bit_cast(bf16x8, pv);
#pragma unroll
      for (int c = 0; c < 4; ++c) {
        o[c] = MFMA32(fr[step & 1][c], pf, o[c]);
        if (step + 1 < 8) ldv(step + 1, c);
        else ldk(0, c);
        if (step + 2 < 8) { if (c > 0) packw(step + 2, c - 1); expo(step + 2, c); }
        if (!(step & 1) && c == 0) early = fl[F_KVL];
        SBAR();
      }
      ol = MFMA32(ones, pf, ol);
      if (step + 2 < 8) packw(step + 2, 3);
      SBAR();
    }
#pragma unroll
    for (int i = 0; i < 8; ++i) a.v[i] += (ch & 1) ? 65536 : -65536;
    if (ch == 0 && pend_flag) {
      WAIT_VM(0);
      if (lds_add_ret(fl + F_OC, lane) == 2 && lane == 0) gst(pend_flag, tag);
    }
  }
  const float inv = 1.0f / ol[0];
  wait4(fl + F_KVP, olds_need);
  if (qnext) {
#pragma unroll
    for (int s = 0; s < 8; ++s) qf[s] = *(const LAS bf16x8*)(qnext + s * 1024 + lane * 16);
#pragma unroll
    for (int s = 0; s < 8; ++s) PIN(qf[s]);
    WAIT_LGKM0();
  }
#pragma unroll
  for (int c = 0; c < 4; ++c) {
#pragma unroll
    for (int r = 0; r < 16; ++r) o[c][r] *= inv;
    const bf16x8 f0 = pack8<false>(o[c], 0), f1 = pack8<false>(o[c], 1);
    *(LAS bf16x8*)(olds + (2 * c) * 1024 + lane * 16) = f0;
    *(LAS bf16x8*)(olds + (2 * c + 1) * 1024 + lane * 16) = f1;
    __builtin_amdgcn_raw_buffer_store_b128(__builtin_bit_cast(u32x4, f0), so, ooff + lane * 16 + (2 * c) * 1024, 0, 16);
    __builtin_amdgcn_raw_buffer_store_b128(__builtin_bit_cast(u32x4, f1), so, ooff + lane * 16 + (2 * c + 1) * 1024, 0, 16);
  }
  WAIT_LGKM0();
  lds_add_ret(olds_cnt, lane);
}

__global__ __launch_bounds__(512, 2) void mha_fused(const char* __restrict__ x, const char* __restrict__ kc, const char* __restrict__ vc, const char* __restrict__ wq,
                                                     const char* __restrict__ wo, unsigned short* __restrict__ out, char* __restrict__ ws) {
  extern __shared__ __attribute__((aligned(16))) char lds[];
  const int tid = threadIdx.x, lane = tid & 63, wave = rfl(tid >> 6);
  const int bid = blockIdx.x, xcd = bid & 7, idx = bid >> 3;
  const int hg = xcd >> 1, hl = idx & 3;
  const int b = 8 * (xcd & 1) + (idx >> 2), h0 = 4 * hg + hl;
  unsigned long long* dbg = (unsigned long long*)(ws + (128u << 20)); (void)dbg;
  unsigned* gf = (unsigned*)(ws + WS_F);
  if (wave == 0) STAMP(0);
  lds_flag fl = (lds_flag)(lds + LDS_FLAG);
  const unsigned lds0 = (unsigned)(uintptr_t)(LAS char*)lds;
  if (tid < 64) fl[tid] = (tid == F_KVP + 3) ? 0x7fffffffu : 0u;
  RAWBAR();

  const __amdgpu_buffer_rsrc_t sows = __builtin_amdgcn_make_buffer_rsrc((void*)ws, 0, WS_P + WS_P_BYTES, 0x00020000);
  const __amdgpu_buffer_rsrc_t swo = __builtin_amdgcn_make_buffer_rsrc((void*)wo, 0, HD * D * 2, 0x00020000);
  auto head_of = [&](int r, int i) { return 16 * r + 4 * hg + ((i + hl) & 3); };
  unsigned wv[5];
#pragma unroll
  for (int j = 0; j < 5; ++j) { const int pi = 64 * j + lane; wv[j] = (pi / 20) * 1280 + (pi % 20) * 16 - (j < 4 ? 1024 * j : 0); }
  auto issue_w = [&](int h, int buf, int part) {
    const unsigned sb = (unsigned)(h * 128 + 16 * part) * 1280u + hl * 320u, dst = lds0 + buf + part * 5120;
    dma<4, 0>(swo, sb, dst, wv[0], wv[1], wv[2], wv[3]); dma<1, 0>(swo, sb, dst + 4096, wv[4]);
  };

  if (wave == 7) {
    __builtin_amdgcn_s_setprio(3);
    const __amdgpu_buffer_rsrc_t sk = __builtin_amdgcn_make_buffer_rsrc((void*)kc, 0, B * H * SK * DH * 2, 0x00020000);
    const __amdgpu_buffer_rsrc_t sv = __builtin_amdgcn_make_buffer_rsrc((void*)vc, 0, B * H * SK * DH * 2, 0x00020000);
    const unsigned v0 = dma_voff<256>(lane);
    auto kvunit = [&](int u) {
      const int c = u >> 3, head = h0 + 16 * (c >> 2), isv = (u >> 2) & 1;
      const unsigned sbase = (unsigned)((b * H + head) * SK + (c & 3) * 128 + (u & 3) * 32) * 256u;
      const unsigned dst = lds0 + ((c & 1) ? 0 : 65536) + (u & 7) * 8192;
      dma<4, 1>(isv ? sv : sk, sbase, dst, v0, v0 ^ 16, v0 ^ 32, v0 ^ 48);
      dma<4, 1>(isv ? sv : sk, sbase + 4096, dst + 4096, v0, v0 ^ 16, v0 ^ 32, v0 ^ 48);
    };
#pragma unroll 1
    for (int t = 0; t < 10; ++t) RAWBAR();
    unsigned freed = 0;
    int P = 0;
#pragma unroll 1
    for (int u = 0; u < 64; ++u) {
      const unsigned need = u < 8 ? 0 : u < 16 ? 1 : u - 15;
      if (freed < need) {
        freed = min4(fl + F_KVP);
        if (freed < need) {
          if (P < u) { WAIT_VM(0); P = u; fl[F_KVL] = P; }
          wait4(fl + F_KVP, need);
          freed = need;
        }
      }
      kvunit(u);
      if (u == 1 || u == 3) RAWBAR();
      WAIT_VM(24);
      if (P < u - 2) { P = u - 2; fl[F_KVL] = P; }
    }
    WAIT_VM(8); fl[F_KVL] = 63;
    WAIT_VM(0); fl[F_KVL] = 64;
    __builtin_amdgcn_s_setprio(0);
    STAMP(5);
  } else if (wave <= 3) {
    f32x16 acc[2][3] = {};
    qproj((lds_cptr)lds, wave, lane, acc);
#pragma unroll
    for (int d = 0; d < 2; ++d)
#pragma unroll
      for (int qb = 0; qb < 3; ++qb) {
        LAS char* qx = (LAS char*)lds + ((wave >> 1) ? LDS_QB : LDS_Q0) + qb * 8192 + (2 * (2 * (wave & 1) + d)) * 1024 + lane * 16;
        *(LAS bf16x8*)qx = pack8<true>(acc[d][qb], 0);
        *(LAS bf16x8*)(qx + 1024) = pack8<true>(acc[d][qb], 1);
      }
    RAWBAR();
  } else if (wave == 4) {
    const unsigned tag0 = gld(gf + FO_EPOCH) + 1u;
    fl[F_EP] = tag0;
    if (lane == 0) gst(gf + FO_SEEN + bid * 16, tag0);
#pragma unroll 1
    for (int t = 0; t < 9; ++t) RAWBAR();
    unsigned wrm = 0;
#pragma unroll
    for (int i = 0; i < 5; ++i) {
      const int L = idx * 320 + i * 64 + lane, hh = L / 1280;
      wrm ^= *(const unsigned*)(wo + (size_t)(16 * (hh >> 2) + 4 * hg + (hh & 3)) * 163840 + (size_t)(L - hh * 1280) * 128);
    }
#pragma unroll 1
    for (int t = 9; t < 12; ++t) RAWBAR();
    asm volatile("" :: "v"(wrm));
    if (bid == 0) {
#pragma unroll
      for (int i = 0; i < 4; ++i) gwait(gf + FO_SEEN + (i * 64 + lane) * 16, tag0);
      if (lane == 0) gst(gf + FO_EPOCH, tag0);
    }
  } else {
    __builtin_amdgcn_s_setprio(3);
    const int pt = wave - 5;
    const int c0 = ((b & 7) + 2 * hl) % 10;
    const __amdgpu_buffer_rsrc_t sw = __builtin_amdgcn_make_buffer_rsrc((void*)wq, 0, D * HD * 2, 0x00020000);
    const __amdgpu_buffer_rsrc_t sx = __builtin_amdgcn_make_buffer_rsrc((void*)x, 0, B * SQ * D * 2, 0x00020000);
    const unsigned v0 = dma_voff<8192>(lane);
    const unsigned xv0 = (lane >> 3) * 1280 + (((lane & 7) ^ (lane >> 4)) * 16);
    auto issue = [&](int t) {
      const int cc = t + c0, c = cc >= 10 ? cc - 10 : cc;
      const unsigned sbase = (unsigned)(c * 64) * 8192u + (h0 + 16 * pt) * 256u;
      const unsigned dst = lds0 + wslot(t);
      const unsigned xs = (unsigned)(b * SQ + 48 * pt) * 1280u + c * 128u;
      dma<4, 0>(sx, xs, dst + 32768 + 6 * pt * 1024, xv0, (xv0 ^ 64) + 9216, xv0 + 2 * 9216, (xv0 ^ 64) + 3 * 9216);
      dma<2, 0>(sx, xs + 4 * 10240, dst + 32768 + (6 * pt + 4) * 1024, xv0, (xv0 ^ 64) + 9216);
#pragma unroll
      for (int j = 0; j < 4; ++j)
        dma<4, 0>(sw, sbase + j * 4 * 32768, dst + pt * 16384 + j * 4096, v0, (v0 ^ 16) + 31744, (v0 ^ 32) + 2 * 31744, (v0 ^ 48) + 3 * 31744);
    };
    issue(0); issue(1);
    WAIT_VM(22); issue(2); RAWBAR();
#pragma unroll 1
    for (int t = 0; t < 10; ++t) {
      if (t < 8) WAIT_VM(22); else WAIT_VM(0);
      RAWBAR();
      if (t + 3 < 10) issue(t + 3);
    }
    RAWBAR();
    __builtin_amdgcn_s_setprio(0);
  }
  const unsigned tag = rfl(fl[F_EP]);

  if (wave <= 2) {
    __builtin_amdgcn_s_setprio(1);
    const int qb = wave;
    bf16x8 qf[8];
    if (wave == 0) STAMP(1);
    AAddr aa; aaddr(aa, (lds_cptr)lds + 65536, lane);
    {
      lds_cptr qx = (lds_cptr)lds + LDS_Q0 + qb * 8192;
#pragma unroll
      for (int s = 0; s < 8; ++s) qf[s] = *(const LAS bf16x8*)(qx + s * 1024 + lane * 16);
#pragma unroll
      for (int s = 0; s < 8; ++s) PIN(qf[s]);
      WAIT_LGKM0();
    }
#pragma unroll 1
    for (int r = 0; r < 2; ++r) {
      attn_head(aa, fl, 32 * r, fl + F_KVP + qb, lane, qf, r == 0 ? (lds_cptr)lds + LDS_QB + qb * 8192 : (lds_cptr) nullptr,
                sows, (unsigned)(((b * H + h0 + 16 * r) * 3 + qb) * 8192), r == 1 ? gf + FO_O + (b * H + h0) * 32 : nullptr, tag,
                (LAS char*)lds + (r ? T_O1 : LDS_QB) + qb * 8192, r ? 56u : 0u, fl + F_OL + r);
      if (wave == 0) STAMP(2 + 2 * r);
    }
    __builtin_amdgcn_s_setprio(0);
  } else {
    if (wave == 3) {
      wait1(0, fl + F_OL, 3);
      gwait(gf + FO_O + (b * H + head_of(0, lane & 3)) * 32, tag);
    } else if (wave < 7) {
      wait4(fl + F_KVP, 56);
#pragma unroll
      for (int p = 0; p < PRE_PARTS; ++p) if (3 * p + (wave - 4) < 8) issue_w(head_of(0, 0), T_W0, 3 * p + (wave - 4));
      WAIT_VM(0);
    }
  }

  {
    const int g = wave & 3, kh = wave >> 2;
    const int hi = lane >> 5, blk = (lane >> 4) & 1, q = (lane & 15) >> 2, p = lane & 3;
    const int aoff = 320 * (64 * kh + 4 * hi + q) + 32 * blk + 8 * p + 64 * g;
    const int gx = g < 3 ? g : 0;
    f32x16 acc[4] = {};
    auto issue_o = [&](int h, int slot) {
      dma<3, 2>(sows, (unsigned)((b * H + h) * 24576 + wave * 3072), lds0 + slot + wave * 3072, lane * 16, lane * 16, lane * 16);
    };
    auto head = [&](int wbuf, int oslot) {
      lds_cptr a0 = (lds_cptr)lds + wbuf + aoff, a1 = a0 + 64 * (4 - g);
      lds_cptr ob = (lds_cptr)lds + oslot + kh * 4096 + lane * 16;
      bf16x8 af[2][2], bf[2][4];
      auto lda = [&](int buf, int k, int i) { lds_cptr o = (i ? a1 : a0) + 320 * 16 * k; af[buf][i] = cat(trd(o), trd(o + 320 * 8)); };
      auto ldb = [&](int buf, int k, int i) { bf[buf][i] = *(const LAS bf16x8*)(ob + (i < 3 ? i : gx) * 8192 + k * 1024); };
      lda(0, 0, 0); ldb(0, 0, 0); ldb(0, 0, 1); ldb(0, 0, 2); lda(0, 0, 1); ldb(0, 0, 3);
#pragma unroll
      for (int k = 0; k < 4; ++k) {
        SBAR();
#pragma unroll
        for (int i = 0; i < 4; ++i) {
          acc[i] = MFMA32(af[k & 1][i == 3], bf[k & 1][i], acc[i]);
          if (k < 3) { ldb((k + 1) & 1, k + 1, i); if (i < 2) lda((k + 1) & 1, k + 1, i); }
          SBAR();
        }
      }
    };
    const int sr[8] = {0, 0, 1, 0, 0, 1, 1, 1}, si[8] = {0, 1, 0, 2, 3, 1, 2, 3};
    const int sl[8] = {LDS_QB, T_OA, T_O1, LDS_QB, T_OA, T_O1, LDS_QB, T_OA};
    RAWBAR();
    if (wave == 0) STAMP(6);
    issue_o(head_of(0, 1), T_OA);
    unsigned pollv = 0;
#pragma unroll
    for (int s = 0; s < 8; ++s) {
      if (s + 1 < 8) issue_w(head_of(sr[s + 1], si[s + 1]), ((s + 1) & 1) ? T_W1 : T_W0, wave);
      if (s >= 1 && s + 2 < 8) issue_o(head_of(sr[s + 2], si[s + 2]), sl[s + 2]);
      if (s == 0 && wave <= 2) {
        WAIT_VM(8);
        if (lds_add_ret(fl + F_OC + 1, lane) == 2 && lane == 0) gst(gf + FO_O + (b * H + h0 + 16) * 32, tag);
      }
      if (s == 1 && wave == 3) pollv = gld(gf + FO_O + (b * H + head_of(1, lane & 3)) * 32);
      head((s & 1) ? T_W1 : T_W0, sl[s]);
      if (s == 2 && wave == 3) {
        if (!__all(pollv == tag)) gwait(gf + FO_O + (b * H + head_of(1, lane & 3)) * 32, tag);
      }
      if (s + 1 < 8) {
        if (s >= 1 && s + 2 < 8) WAIT_VM(3); else WAIT_VM(0);
        RAWBAR();
      }
    }
    if (wave == 0) STAMP(7);
    RAWBAR();
    {
      LAS char* xch = (LAS char*)lds + wave * 8192 + lane * 16;
#pragma unroll
      for (int i = 0; i < 2; ++i)
#pragma unroll
        for (int j = 0; j < 4; ++j) { const f32x16& t = acc[kh ? i : 2 + i]; f32x4 v = {t[4 * j], t[4 * j + 1], t[4 * j + 2], t[4 * j + 3]}; *(LAS f32x4*)(xch + (i * 4 + j) * 1024) = v; }
      RAWBAR();
      lds_cptr xr = (lds_cptr)lds + (wave ^ 4) * 8192 + lane * 16;
#pragma unroll
      for (int i = 0; i < 2; ++i)
#pragma unroll
        for (int j = 0; j < 4; ++j) { const f32x4 v = *(const LAS f32x4*)(xr + (i * 4 + j) * 1024); f32x16& t = acc[kh ? 2 + i : i];
          t[4 * j] += v[0]; t[4 * j + 1] += v[1]; t[4 * j + 2] += v[2]; t[4 * j + 3] += v[3]; }
    }
    const unsigned pbase = WS_P + (unsigned)((b * 4 + hl) * 4) * (15 * 4096);
#pragma unroll
    for (int ii = 0; ii < 2; ++ii) {
      const int i = 2 * kh + ii, tile = i < 3 ? g * 3 + i : 12 + g;
      if ((i < 3 || g < 3) && (tile & 3) != hg) {
        const f32x16& t = kh ? acc[2 + ii] : acc[ii];
#pragma unroll
        for (int j = 0; j < 4; ++j) {
          f32x4 v = {t[4 * j], t[4 * j + 1], t[4 * j + 2], t[4 * j + 3]};
          __builtin_amdgcn_raw_buffer_store_b128(__builtin_bit_cast(u32x4, v), sows, pbase + (hg * 15 + tile) * 4096 + lane * 16 + j * 1024, 0, 16);
        }
      }
    }
    WAIT_VM(0);
    if (lane == 0) gst(gf + FO_P + (((b * 4 + hl) * 4 + hg) * 8 + wave) * 32, tag);
    gwait(gf + FO_P + (((b * 4 + hl) * 4 + ((hg + 1 + (lane % 3)) & 3)) * 8 + wave) * 32, tag);
#pragma unroll
    for (int ii = 0; ii < 2; ++ii) {
      const int i = 2 * kh + ii, tile = i < 3 ? g * 3 + i : 12 + g;
      if ((i < 3 || g < 3) && (tile & 3) == hg) {
        const f32x16& t = kh ? acc[2 + ii] : acc[ii];
        u32x4 pv[3][4];
#pragma unroll
        for (int o = 0; o < 3; ++o)
#pragma unroll
          for (int j = 0; j < 4; ++j) pv[o][j] = __builtin_amdgcn_raw_buffer_load_b128(sows, lane * 16 + j * 1024, pbase + (((hg + 1 + o) & 3) * 15 + tile) * 4096, 16);
        const int nt = i < 3 ? g : 4, qb = i < 3 ? i : g;
        unsigned short* orow = out + (size_t)(b * SQ + 32 * qb + (lane & 31)) * D + 160 * hl + 32 * nt + 4 * hi;
#pragma unroll
        for (int j = 0; j < 4; ++j) {
          f32x4 v = {t[4 * j], t[4 * j + 1], t[4 * j + 2], t[4 * j + 3]};
#pragma unroll
          for (int o = 0; o < 3; ++o) v += __builtin_bit_cast(f32x4, pv[o][j]);
          u32x2 w2 = {cvtpk(v[0], v[1]), cvtpk(v[2], v[3])};
          *(u32x2*)(orow + 8 * j) = w2;
        }
      }
    }
    if (wave == 0) STAMP(8);
  }
}

extern "C" void kernel_launch(void* const* d_in, const int* in_sizes, int n_in, void* d_out, int out_size, void* d_ws, size_t ws_size, hipStream_t stream) {
  (void)hipFuncSetAttribute((const void*)mha_fused, hipFuncAttributeMaxDynamicSharedMemorySize, LDS_BYTES);
  mha_fused<<<256, 512, LDS_BYTES, stream>>>((const char*)d_in[0], (const char*)d_in[1], (const char*)d_in[2], (const char*)d_in[3], (const char*)d_in[4],
                                              (unsigned short*)d_out, (char*)d_ws);
}
```

```cpp
#include <hip/hip_runtime.h>

typedef short bf16x8 __attribute__((ext_vector_type(8)));
typedef short s16x4 __attribute__((ext_vector_type(4)));
typedef float f32x16 __attribute__((ext_vector_type(16)));
typedef float f32x4 __attribute__((ext_vector_type(4)));
typedef float f32x2 __attribute__((ext_vector_type(2)));
typedef unsigned u32x4 __attribute__((ext_vector_type(4)));
typedef unsigned u32x2 __attribute__((ext_vector_type(2)));
typedef __bf16 bf16x2n __attribute__((ext_vector_type(2)));
#define LAS __attribute__((address_space(3)))
typedef LAS const char* lds_cptr;
typedef volatile LAS unsigned* lds_flag;

constexpr int B = 16, SQ = 96, D = 640, H = 32, SK = 512, DH = 128, HD = H * DH;
constexpr int SLOT = 16384;
constexpr int WSLOT = 45056;
constexpr int LDS_Q0 = 0, LDS_QB = 131072  , LDS_FLAG = 155648, LDS_BYTES = LDS_FLAG + 256;
constexpr int T_W0 = 65536, T_W1 = 0, T_O1 = 106496, T_OA = 40960;
constexpr int F_KVL = 0, F_KVP = 4, F_EP = 8, F_OC = 12, F_OL = 16;
constexpr unsigned WS_P = 16u << 20, WS_P_BYTES = 16 * 4 * 4 * 15 * 4096, WS_F = 40u << 20;
constexpr unsigned FO_EPOCH = 0, FO_SEEN = 64, FO_O = FO_SEEN + 256 * 16, FO_P = FO_O + 512 * 32;
constexpr float CS = 0.088388347648318447f * 1.4426950408889634f;
constexpr int PRE_PARTS = 3;
constexpr float THR = 8.0f;

#define STAMP(k)

#define MFMA32(a, b, c) __builtin_amdgcn_mfma_f32_32x32x16_bf16(a, b, c, 0, 0, 0)
#define SBAR() __builtin_amdgcn_sched_barrier(0)
#define MEMBAR() asm volatile("" ::: "memory")
#define PIN(x) asm volatile("" : "+v"(x))
#define DI __device__ __forceinline__
#define WAIT_VM(n) asm volatile("s_waitcnt vmcnt(" #n ")" ::: "memory")
#define WAIT_LGKM0() asm volatile("s_waitcnt lgkmcnt(0)" ::: "memory")
#define RAWBAR() do { WAIT_LGKM0(); __builtin_amdgcn_s_barrier(); MEMBAR(); } while (0)

DI unsigned cvtpk(float lo, float hi) { f32x2 v = {lo, hi}; return __builtin_bit_cast(unsigned, __builtin_convertvector(v, bf16x2n)); }
DI s16x4 trd(lds_cptr p) { return __builtin_amdgcn_ds_read_tr16_b64_v4i16((LAS s16x4*)p); }
DI bf16x8 cat(s16x4 a, s16x4 b) { return __builtin_shufflevector(a, b, 0, 1, 2, 3, 4, 5, 6, 7); }
template <bool SCALE> DI bf16x8 pack8(const f32x16& x, int s) {
  const float c = SCALE ? CS : 1.0f;
  u32x4 w = {cvtpk(x[8 * s] * c, x[8 * s + 1] * c), cvtpk(x[8 * s + 2] * c, x[8 * s + 3] * c), cvtpk(x[8 * s + 4] * c, x[8 * s + 5] * c), cvtpk(x[8 * s + 6] * c, x[8 * s + 7] * c)};
  return __builtin_bit_cast(bf16x8, w);
}
DI unsigned rfl(unsigned v) { return __builtin_amdgcn_readfirstlane(v); }
DI unsigned lds_addr(lds_flag f) { return (unsigned)(uintptr_t)f; }

DI void wait1(unsigned early, lds_flag f, unsigned need) {
  unsigned t, sv, cnt;
  asm volatile("s_cmp_ge_u32 %3, %4\n\ts_cbranch_scc1 W1E%=\n\ts_mov_b32 %2, 0\n"
               "W1L%=:\n\tds_read_b32 %0, %5\n\ts_waitcnt lgkmcnt(0)\n\tv_readfirstlane_b32 %1, %0\n\ts_cmp_ge_u32 %1, %4\n\ts_cbranch_scc1 W1E%=\n\t"
               "s_sleep 1\n\ts_add_u32 %2, %2, 1\n\ts_cmp_lt_u32 %2, 0x40000\n\ts_cbranch_scc1 W1L%=\n"
               "W1E%=:"
               : "=&v"(t), "=&s"(sv), "=&s"(cnt) : "s"(early), "s"(need), "v"(lds_addr(f)) : "memory", "scc");
}
DI void wait4(lds_flag f, unsigned need) {
  unsigned a0, a1, a2, m, sv, cnt;
  asm volatile("s_mov_b32 %5, 0\n"
               "W4L%=:\n\tds_read_b32 %3, %7\n\tds_read_b32 %0, %7 offset:4\n\tds_read_b32 %1, %7 offset:8\n\tds_read_b32 %2, %7 offset:12\n\ts_waitcnt lgkmcnt(0)\n\t"
               "v_min_u32 %3, %3, %0\n\tv_min3_u32 %3, %3, %1, %2\n\ts_nop 0\n\tv_readfirstlane_b32 %4, %3\n\ts_cmp_ge_u32 %4, %6\n\ts_cbranch_scc1 W4E%=\n\t"
               "s_sleep 1\n\ts_add_u32 %5, %5, 1\n\ts_cmp_lt_u32 %5, 0x40000\n\ts_cbranch_scc1 W4L%=\n"
               "W4E%=:"
               : "=&v"(a0), "=&v"(a1), "=&v"(a2), "=&v"(m), "=&s"(sv), "=&s"(cnt) : "s"(need), "v"(lds_addr(f)) : "memory", "scc");
}
DI unsigned min4(lds_flag f) { u32x4 v = *(volatile LAS u32x4*)f; unsigned m = v[0] < v[1] ? v[0] : v[1]; m = m < v[2] ? m : v[2]; return rfl(m < v[3] ? m : v[3]); }
DI unsigned lds_add_ret(lds_flag f, int lane) {
  unsigned old = 0;
  if (lane == 0) old = __hip_atomic_fetch_add((LAS unsigned*)f, 1u, __ATOMIC_RELAXED, __HIP_MEMORY_SCOPE_WORKGROUP);
  return rfl(old);
}
DI unsigned gld(const unsigned* p) { return __hip_atomic_load(p, __ATOMIC_RELAXED, __HIP_MEMORY_SCOPE_AGENT); }
DI void gst(unsigned* p, unsigned v) { __hip_atomic_store(p, v, __ATOMIC_RELAXED, __HIP_MEMORY_SCOPE_AGENT); }
DI void gwait(const unsigned* p, unsigned tag) {
  for (unsigned spins = 0; spins < (1u << 15); ++spins) {
    if (__all(gld(p) == tag)) break;
    __builtin_amdgcn_s_sleep(2);
  }
  MEMBAR();
}

#define DMA_HEAD "s_nop 2\n\ts_mov_b32 m0, %0\n\ts_nop 0\n\t"
#define DMA_BODY(POL) \
  if constexpr (N == 1) asm volatile(DMA_HEAD "buffer_load_dwordx4 %1, %2, %3 offen" POL " lds" ::"s"(dst), "v"(va), "s"(srd), "s"(soff) : "m0", "memory"); \
  else if constexpr (N == 2) asm volatile(DMA_HEAD "buffer_load_dwordx4 %1, %3, %4 offen" POL " lds\n\tbuffer_load_dwordx4 %2, %3, %4 offen offset:1024" POL " lds" \
                                          ::"s"(dst), "v"(va), "v"(vb), "s"(srd), "s"(soff) : "m0", "memory"); \
  else if constexpr (N == 3) asm volatile(DMA_HEAD "buffer_load_dwordx4 %1, %4, %5 offen" POL " lds\n\tbuffer_load_dwordx4 %2, %4, %5 offen offset:1024" POL " lds\n\t" \
                                          "buffer_load_dwordx4 %3, %4, %5 offen offset:2048" POL " lds" ::"s"(dst), "v"(va), "v"(vb), "v"(vc), "s"(srd), "s"(soff) : "m0", "memory"); \
  else asm volatile(DMA_HEAD "buffer_load_dwordx4 %1, %5, %6 offen" POL " lds\n\tbuffer_load_dwordx4 %2, %5, %6 offen offset:1024" POL " lds\n\t" \
                    "buffer_load_dwordx4 %3, %5, %6 offen offset:2048" POL " lds\n\tbuffer_load_dwordx4 %4, %5, %6 offen offset:3072" POL " lds" \
                    ::"s"(dst), "v"(va), "v"(vb), "v"(vc), "v"(vd), "s"(srd), "s"(soff) : "m0", "memory");
template <int N, int POL>
DI void dma(__amdgpu_buffer_rsrc_t srd, unsigned soff, unsigned dst, unsigned va, unsigned vb = 0, unsigned vc = 0, unsigned vd = 0) {
  dst = rfl(dst); soff = rfl(soff);
  if constexpr (POL == 0) { DMA_BODY("") } else if constexpr (POL == 1) { DMA_BODY(" nt") } else { DMA_BODY(" sc1") }
}
template <int PITCH> DI unsigned dma_voff(int i) { return (i >> 4) * PITCH + 16 * ((i & 15) ^ ((i >> 4) << 2)); }

DI int wslot(int t) { return (t % 3) * WSLOT; }
DI void qproj(lds_cptr lds, int i, int lane, f32x16 (&acc)[2][3]) {
  const int hi = lane >> 5, blk = (lane >> 4) & 1, q = (lane & 15) >> 2, pp = lane & 3, r32 = lane & 31;
  lds_cptr aw[4], ax[4];
#pragma unroll
  for (int d = 0; d < 2; ++d)
#pragma unroll
    for (int t = 0; t < 2; ++t)
      aw[2 * d + t] = lds + 16384 * (i >> 1) + 2048 * hi + 1024 * t + 256 * q + 16 * (4 * ((2 * (i & 1) + d) ^ q) + ((2 * blk + (pp & 1)) ^ (2 * hi + t))) + 8 * (pp >> 1);
#pragma unroll
  for (int u = 0; u < 4; ++u) ax[u] = lds + 32768 + 128 * r32 + 16 * ((2 * u + hi) ^ ((r32 >> 1) & 7));
  bf16x8 wf[2][2], xf[2][3];
  auto ldw = [&](int buf, int so, int u, int d) { wf[buf][d] = cat(trd(aw[2 * d] + so + 4096 * u), trd(aw[2 * d + 1] + so + 4096 * u)); };
  auto ldx = [&](int buf, int so, int u, int qb) { xf[buf][qb] = *(const LAS bf16x8*)(ax[u] + so + 4096 * qb); };
  RAWBAR();
  int so = wslot(0);
#pragma unroll
  for (int d = 0; d < 2; ++d) ldw(0, so, 0, d);
#pragma unroll
  for (int qb = 0; qb < 3; ++qb) ldx(0, so, 0, qb);
#pragma unroll 1
  for (int t = 0; t < 10; ++t) {
    const int son = wslot(t + 1);
#pragma unroll
    for (int u = 0; u < 4; ++u) {
      SBAR();
      const int nb = (u + 1) & 1, nso = u < 3 ? so : son, nu = (u + 1) & 3;
#pragma unroll
      for (int g = 0; g < 6; ++g) {
        const int d = g / 3, qb = g % 3;
        acc[d][qb] = MFMA32(wf[u & 1][d], xf[u & 1][qb], acc[d][qb]);
        if (u < 3) {
          if (g == 0) ldw(nb, nso, nu, 0);
          else if (g < 4) ldx(nb, nso, nu, g - 1);
          else if (g == 4) ldw(nb, nso, nu, 1);
        } else {
          if (g == 2) { SBAR(); RAWBAR(); ldw(nb, nso, nu, 0); ldx(nb, nso, nu, 0); }
          else if (g == 3) { ldx(nb, nso, nu, 1); ldx(nb, nso, nu, 2); }
          else if (g == 4) ldw(nb, nso, nu, 1);
        }
        SBAR();
      }
    }
    so = son;
  }
}

struct AAddr { lds_cptr k[8]; lds_cptr v[8]; };
DI void aaddr(AAddr& a, lds_cptr kv, int lane) {
  const int r32 = lane & 31, hi = lane >> 5, blk = (lane >> 4) & 1, q = (lane & 15) >> 2, p = lane & 3;
  const int f = ((r32 & 3) << 2) | ((r32 >> 2) & 3);
#pragma unroll
  for (int s = 0; s < 8; ++s) a.k[s] = kv + 256 * r32 + 16 * ((2 * s + hi) ^ f);
#pragma unroll
  for (int c = 0; c < 4; ++c)
#pragma unroll
    for (int t = 0; t < 2; ++t)
      a.v[2 * c + t] = kv + 2 * SLOT + 2048 * t + 1024 * hi + 256 * q + 16 * (4 * (c ^ q) + ((2 * blk + (p >> 1)) ^ (2 * t + hi))) + 8 * (p & 1);
}
DI float max3(float a, float b, float c) { return __builtin_fmaxf(__builtin_fmaxf(a, b), c); }
DI void attn_head(AAddr& a, lds_flag fl, int n0  , lds_flag prog, int lane, bf16x8 (&qf)[8], lds_cptr qnext,
                  __amdgpu_buffer_rsrc_t so, unsigned ooff, unsigned* pend_flag, unsigned tag, LAS char* olds  , unsigned olds_need, lds_flag olds_cnt) {
  f32x16 o[4] = {};
  f32x16 ol = {};
  f32x16 negr = {};
  const bf16x8 ones = {0x3F80, 0x3F80, 0x3F80, 0x3F80, 0x3F80, 0x3F80, 0x3F80, 0x3F80};
  bf16x8 fr[2][4];
  auto ldk = [&](int step, int i) {
    const int tt = step >> 2, kb = (step >> 1) & 1, hf = step & 1;
    fr[step & 1][i] = *(const LAS bf16x8*)(a.k[4 * hf + i] + SLOT * tt + 8192 * kb);
  };
  auto ldv = [&](int step, int c) {
    const int tt = step >> 2, ks = step & 3;
    fr[step & 1][c] = cat(trd(a.v[2 * c] + SLOT * tt + 4096 * ks), trd(a.v[2 * c + 1] + SLOT * tt + 4096 * ks));
  };
  wait1(0, fl + F_KVL, n0 + 1);
#pragma unroll
  for (int i = 0; i < 4; ++i) ldk(0, i);
#pragma unroll 1
  for (int ch = 0; ch < 4; ++ch) {
    const int n = n0 + 8 * ch;
    f32x16 s[4];
    unsigned early = 0;
    float mx = -__builtin_inff();
#pragma unroll
    for (int step = 0; step < 8; ++step) {
      if (step & 1) { MEMBAR(); prog[0] = n + (step >> 1) + 1; if (step < 7) wait1(rfl(early), fl + F_KVL, n + (step >> 1) + 2); }
      SBAR();
      const int ti = step >> 1, hf = step & 1;
#pragma unroll
      for (int i = 0; i < 4; ++i) {
        if (hf == 0 && i == 0) s[ti] = MFMA32(fr[step & 1][0], qf[0], negr);
        else s[ti] = MFMA32(fr[step & 1][i], qf[4 * hf + i], s[ti]);
        if (step + 1 < 8) ldk(step + 1, i);
        if ((!(step & 1) || step == 7) && i == 0) early = fl[F_KVL];
        if (ti >= 1 && (hf == 1 || i >= 2)) {
          const int g = hf == 0 ? i - 2 : 2 + i;
          const f32x16& x = s[ti - 1];
          if (g < 2) { mx = max3(mx, x[4 * g], x[4 * g + 1]); mx = max3(mx, x[4 * g + 2], x[4 * g + 3]); }
          else mx = max3(mx, x[8 + 2 * (g - 2)], x[9 + 2 * (g - 2)]);
          PIN(mx);
        }
        SBAR();
      }
    }
#pragma unroll
    for (int i = 0; i < 8; ++i) a.k[i] += (ch & 1) ? 65536 : -65536;
    wait1(rfl(early), fl + F_KVL, n + 5);
#pragma unroll
    for (int c = 0; c < 4; ++c) ldv(0, c);
    SBAR();
#pragma unroll
    for (int r = 0; r < 16; r += 2) mx = max3(mx, s[3][r], s[3][r + 1]);
    { auto rr = __builtin_amdgcn_permlane32_swap(__float_as_uint(mx), __float_as_uint(mx), false, false);
      mx = __builtin_fmaxf(__uint_as_float(rr[0]), __uint_as_float(rr[1])); }
    if (ch == 0 || __any(mx > THR)) {
      const float dl = ch == 0 ? mx : __builtin_fmaxf(mx, 0.f);
#pragma unroll
      for (int i = 0; i < 4; ++i)
#pragma unroll
        for (int r = 0; r < 16; ++r) s[i][r] -= dl;
#pragma unroll
      for (int r = 0; r < 16; ++r) negr[r] -= dl;
      if (ch > 0) {
        const float alpha = __builtin_amdgcn_exp2f(-dl);
        ol[0] *= alpha;
#pragma unroll
        for (int c = 0; c < 4; ++c)
#pragma unroll
          for (int r = 0; r < 16; ++r) o[c][r] *= alpha;
      }
    }
    unsigned pw[3][4];
    float pe[2][2];
    auto expo = [&](int step, int i) {
      const f32x16& x = s[step >> 1];
      const int e = 8 * (step & 1) + 2 * i;
      pe[i & 1][0] = __builtin_amdgcn_exp2f(x[e]); pe[i & 1][1] = __builtin_amdgcn_exp2f(x[e + 1]);
      PIN(pe[i & 1][0]); PIN(pe[i & 1][1]);
    };
    auto packw = [&](int step, int i) { pw[step % 3][i] = cvtpk(pe[i & 1][0], pe[i & 1][1]); PIN(pw[step % 3][i]); };
#pragma unroll
    for (int st = 0; st < 2; ++st)
#pragma unroll
      for (int i = 0; i < 4; ++i) { expo(st, i); packw(st, i); }
    SBAR();
#pragma unroll
    for (int step = 0; step < 8; ++step) {
      if (step & 1) { MEMBAR(); prog[0] = n + 4 + (step >> 1) + 1; wait1(rfl(early), fl + F_KVL, (step < 7 || ch < 3) ? n + 4 + (step >> 1) + 2 : 0); }
      SBAR();
      u32x4 pv = {pw[step % 3][0], pw[step % 3][1], pw[step % 3][2], pw[step % 3][3]};
      const bf16x8 pf = __builtin_bit_cast(bf16x8, pv);
#pragma unroll
      for (int c = 0; c < 4; ++c) {
        o[c] = MFMA32(fr[step & 1][c], pf, o[c]);
        if (step + 1 < 8) ldv(step + 1, c);
        else ldk(0, c);
        if (step + 2 < 8) { if (c > 0) packw(step + 2, c - 1); expo(step + 2, c); }
        if (!(step & 1) && c == 0) early = fl[F_KVL];
        SBAR();
      }
      ol = MFMA32(ones, pf, ol);
      if (step + 2 < 8) packw(step + 2, 3);
      SBAR();
    }
#pragma unroll
    for (int i = 0; i < 8; ++i) a.v[i] += (ch & 1) ? 65536 : -65536;
    if (ch == 0 && pend_flag) {
      WAIT_VM(0);
      if (lds_add_ret(fl + F_OC, lane) == 2 && lane == 0) gst(pend_flag, tag);
    }
  }
  const float inv = 1.0f / ol[0];
  wait4(fl + F_KVP, olds_need);
  if (qnext) {
#pragma unroll
    for (int s = 0; s < 8; ++s) qf[s] = *(const LAS bf16x8*)(qnext + s * 1024 + lane * 16);
#pragma unroll
    for (int s = 0; s < 8; ++s) PIN(qf[s]);
    WAIT_LGKM0();
  }
#pragma unroll
  for (int c = 0; c < 4; ++c) {
#pragma unroll
    for (int r = 0; r < 16; ++r) o[c][r] *= inv;
    const bf16x8 f0 = pack8<false>(o[c], 0), f1 = pack8<false>(o[c], 1);
    *(LAS bf16x8*)(olds + (2 * c) * 1024 + lane * 16) = f0;
    *(LAS bf16x8*)(olds + (2 * c + 1) * 1024 + lane * 16) = f1;
    __builtin_amdgcn_raw_buffer_store_b128(__builtin_bit_cast(u32x4, f0), so, ooff + lane * 16 + (2 * c) * 1024, 0, 16);
    __builtin_amdgcn_raw_buffer_store_b128(__builtin_bit_cast(u32x4, f1), so, ooff + lane * 16 + (2 * c + 1) * 1024, 0, 16);
  }
  WAIT_LGKM0();
  lds_add_ret(olds_cnt, lane);
}

__global__ __launch_bounds__(512, 2) void mha_fused(const char* __restrict__ x, const char* __restrict__ kc, const char* __restrict__ vc, const char* __restrict__ wq,
                                                     const char* __restrict__ wo, unsigned short* __restrict__ out, char* __restrict__ ws) {
  extern __shared__ __attribute__((aligned(16))) char lds[];
  const int tid = threadIdx.x, lane = tid & 63, wave = rfl(tid >> 6);
  const int bid = blockIdx.x, xcd = bid & 7, idx = bid >> 3;
  const int hg = xcd >> 1, hl = idx & 3;
  const int b = 8 * (xcd & 1) + (idx >> 2), h0 = 4 * hg + hl;
  unsigned long long* dbg = (unsigned long long*)(ws + (128u << 20)); (void)dbg;
  unsigned* gf = (unsigned*)(ws + WS_F);
  if (wave == 0) STAMP(0);
  lds_flag fl = (lds_flag)(lds + LDS_FLAG);
  const unsigned lds0 = (unsigned)(uintptr_t)(LAS char*)lds;

  const __amdgpu_buffer_rsrc_t sows = __builtin_amdgcn_make_buffer_rsrc((void*)ws, 0, WS_P + WS_P_BYTES, 0x00020000);
  const __amdgpu_buffer_rsrc_t swo = __builtin_amdgcn_make_buffer_rsrc((void*)wo, 0, HD * D * 2, 0x00020000);
  auto head_of = [&](int r, int i) { return 16 * r + 4 * hg + ((i + hl) & 3); };
  unsigned wv[5];
#pragma unroll
  for (int j = 0; j < 5; ++j) { const int pi = 64 * j + lane; wv[j] = (pi / 20) * 1280 + (pi % 20) * 16 - (j < 4 ? 1024 * j : 0); }
  auto issue_w = [&](int h, int buf, int part) {
    const unsigned sb = (unsigned)(h * 128 + 16 * part) * 1280u + hl * 320u, dst = lds0 + buf + part * 5120;
    dma<4, 0>(swo, sb, dst, wv[0], wv[1], wv[2], wv[3]); dma<1, 0>(swo, sb, dst + 4096, wv[4]);
  };

  if (wave == 7) {
    __builtin_amdgcn_s_setprio(3);
    const __amdgpu_buffer_rsrc_t sk = __builtin_amdgcn_make_buffer_rsrc((void*)kc, 0, B * H * SK * DH * 2, 0x00020000);
    const __amdgpu_buffer_rsrc_t sv = __builtin_amdgcn_make_buffer_rsrc((void*)vc, 0, B * H * SK * DH * 2, 0x00020000);
    const unsigned v0 = dma_voff<256>(lane);
    auto kvunit = [&](int u) {
      const int c = u >> 3, head = h0 + 16 * (c >> 2), isv = (u >> 2) & 1;
      const unsigned sbase = (unsigned)((b * H + head) * SK + (c & 3) * 128 + (u & 3) * 32) * 256u;
      const unsigned dst = lds0 + ((c & 1) ? 0 : 65536) + (u & 7) * 8192;
      dma<4, 1>(isv ? sv : sk, sbase, dst, v0, v0 ^ 16, v0 ^ 32, v0 ^ 48);
      dma<4, 1>(isv ? sv : sk, sbase + 4096, dst + 4096, v0, v0 ^ 16, v0 ^ 32, v0 ^ 48);
    };
#pragma unroll 1
    for (int t = 0; t < 10; ++t) RAWBAR();
    unsigned freed = 0;
    int P = 0;
#pragma unroll 1
    for (int u = 0; u < 64; ++u) {
      const unsigned need = u < 8 ? 0 : u < 16 ? 1 : u - 15;
      if (freed < need) {
        freed = min4(fl + F_KVP);
        if (freed < need) {
          if (P < u) { WAIT_VM(0); P = u; fl[F_KVL] = P; }
          wait4(fl + F_KVP, need);
          freed = need;
        }
      }
      kvunit(u);
      if (u == 1 || u == 3) RAWBAR();
      WAIT_VM(24);
      if (P < u - 2) { P = u - 2; fl[F_KVL] = P; }
    }
    WAIT_VM(8); fl[F_KVL] = 63;
    WAIT_VM(0); fl[F_KVL] = 64;
    __builtin_amdgcn_s_setprio(0);
    STAMP(5);
  } else if (wave <= 3) {
    f32x16 acc[2][3] = {};
    qproj((lds_cptr)lds, wave, lane, acc);
#pragma unroll
    for (int d = 0; d < 2; ++d)
#pragma unroll
      for (int qb = 0; qb < 3; ++qb) {
        LAS char* qx = (LAS char*)lds + ((wave >> 1) ? LDS_QB : LDS_Q0) + qb * 8192 + (2 * (2 * (wave & 1) + d)) * 1024 + lane * 16;
        *(LAS bf16x8*)qx = pack8<true>(acc[d][qb], 0);
        *(LAS bf16x8*)(qx + 1024) = pack8<true>(acc[d][qb], 1);
      }
    RAWBAR();
  } else if (wave == 4) {
    fl[lane] = (lane == F_KVP + 3) ? 0x7fffffffu : 0u;
    const unsigned tag0 = gld(gf + FO_EPOCH) + 1u;
    fl[F_EP] = tag0;
    if (lane == 0) gst(gf + FO_SEEN + bid * 16, tag0);
#pragma unroll 1
    for (int t = 0; t < 9; ++t) RAWBAR();
    unsigned wrm = 0;
#pragma unroll
    for (int i = 0; i < 5; ++i) {
      const int L = idx * 320 + i * 64 + lane, hh = L / 1280;
      wrm ^= *(const unsigned*)(wo + (size_t)(16 * (hh >> 2) + 4 * hg + (hh & 3)) * 163840 + (size_t)(L - hh * 1280) * 128);
    }
#pragma unroll 1
    for (int t = 9; t < 12; ++t) RAWBAR();
    asm volatile("" :: "v"(wrm));
    if (bid == 0) {
#pragma unroll
      for (int i = 0; i < 4; ++i) gwait(gf + FO_SEEN + (i * 64 + lane) * 16, tag0);
      if (lane == 0) gst(gf + FO_EPOCH, tag0);
    }
  } else {
    __builtin_amdgcn_s_setprio(3);
    const int pt = wave - 5;
    const int c0 = ((b & 7) + 2 * hl) % 10;
    const __amdgpu_buffer_rsrc_t sw = __builtin_amdgcn_make_buffer_rsrc((void*)wq, 0, D * HD * 2, 0x00020000);
    const __amdgpu_buffer_rsrc_t sx = __builtin_amdgcn_make_buffer_rsrc((void*)x, 0, B * SQ * D * 2, 0x00020000);
    const unsigned v0 = dma_voff<8192>(lane);
    const unsigned xv0 = (lane >> 3) * 1280 + (((lane & 7) ^ (lane >> 4)) * 16);
    auto issue = [&](int t) {
      const int cc = t + c0, c = cc >= 10 ? cc - 10 : cc;
      const unsigned sbase = (unsigned)(c * 64) * 8192u + (h0 + 16 * pt) * 256u;
      const unsigned dst = lds0 + wslot(t);
      const unsigned xs = (unsigned)(b * SQ + 48 * pt) * 1280u + c * 128u;
      dma<4, 0>(sx, xs, dst + 32768 + 6 * pt * 1024, xv0, (xv0 ^ 64) + 9216, xv0 + 2 * 9216, (xv0 ^ 64) + 3 * 9216);
      dma<2, 0>(sx, xs + 4 * 10240, dst + 32768 + (6 * pt + 4) * 1024, xv0, (xv0 ^ 64) + 9216);
#pragma unroll
      for (int j = 0; j < 4; ++j)
        dma<4, 0>(sw, sbase + j * 4 * 32768, dst + pt * 16384 + j * 4096, v0, (v0 ^ 16) + 31744, (v0 ^ 32) + 2 * 31744, (v0 ^ 48) + 3 * 31744);
    };
    issue(0); issue(1);
    WAIT_VM(22); issue(2); RAWBAR();
#pragma unroll 1
    for (int t = 0; t < 10; ++t) {
      if (t < 8) WAIT_VM(22); else WAIT_VM(0);
      RAWBAR();
      if (t + 3 < 10) issue(t + 3);
    }
    RAWBAR();
    __builtin_amdgcn_s_setprio(0);
  }
  const unsigned tag = rfl(fl[F_EP]);

  if (wave <= 2) {
    __builtin_amdgcn_s_setprio(1);
    const int qb = wave;
    bf16x8 qf[8];
    if (wave == 0) STAMP(1);
    AAddr aa; aaddr(aa, (lds_cptr)lds + 65536, lane);
    {
      lds_cptr qx = (lds_cptr)lds + LDS_Q0 + qb * 8192;
#pragma unroll
      for (int s = 0; s < 8; ++s) qf[s] = *(const LAS bf16x8*)(qx + s * 1024 + lane * 16);
#pragma unroll
      for (int s = 0; s < 8; ++s) PIN(qf[s]);
      WAIT_LGKM0();
    }
#pragma unroll 1
    for (int r = 0; r < 2; ++r) {
      attn_head(aa, fl, 32 * r, fl + F_KVP + qb, lane, qf, r == 0 ? (lds_cptr)lds + LDS_QB + qb * 8192 : (lds_cptr) nullptr,
                sows, (unsigned)(((b * H + h0 + 16 * r) * 3 + qb) * 8192), r == 1 ? gf + FO_O + (b * H + h0) * 32 : nullptr, tag,
                (LAS char*)lds + (r ? T_O1 : LDS_QB) + qb * 8192, r ? 56u : 0u, fl + F_OL + r);
      if (wave == 0) STAMP(2 + 2 * r);
    }
    __builtin_amdgcn_s_setprio(0);
  } else {
    if (wave == 3) {
      wait1(0, fl + F_OL, 3);
      gwait(gf + FO_O + (b * H + head_of(0, lane & 3)) * 32, tag);
    } else if (wave < 7) {
      wait4(fl + F_KVP, 56);
#pragma unroll
      for (int p = 0; p < PRE_PARTS; ++p) if (3 * p + (wave - 4) < 8) issue_w(head_of(0, 0), T_W0, 3 * p + (wave - 4));
      WAIT_VM(0);
    }
  }

  {
    const int g = wave & 3, kh = wave >> 2;
    const int hi = lane >> 5, blk = (lane >> 4) & 1, q = (lane & 15) >> 2, p = lane & 3;
    const int aoff = 320 * (64 * kh + 4 * hi + q) + 32 * blk + 8 * p + 64 * g;
    const int gx = g < 3 ? g : 0;
    f32x16 acc[4] = {};
    auto issue_o = [&](int h, int slot) {
      dma<3, 2>(sows, (unsigned)((b * H + h) * 24576 + wave * 3072), lds0 + slot + wave * 3072, lane * 16, lane * 16, lane * 16);
    };
    auto head = [&](int wbuf, int oslot) {
      lds_cptr a0 = (lds_cptr)lds + wbuf + aoff, a1 = a0 + 64 * (4 - g);
      lds_cptr ob = (lds_cptr)lds + oslot + kh * 4096 + lane * 16;
      bf16x8 af[2][2], bf[2][4];
      auto lda = [&](int buf, int k, int i) { lds_cptr o = (i ? a1 : a0) + 320 * 16 * k; af[buf][i] = cat(trd(o), trd(o + 320 * 8)); };
      auto ldb = [&](int buf, int k, int i) { bf[buf][i] = *(const LAS bf16x8*)(ob + (i < 3 ? i : gx) * 8192 + k * 1024); };
      lda(0, 0, 0); ldb(0, 0, 0); ldb(0, 0, 1); ldb(0, 0, 2); lda(0, 0, 1); ldb(0, 0, 3);
#pragma unroll
      for (int k = 0; k < 4; ++k) {
        SBAR();
#pragma unroll
        for (int i = 0; i < 4; ++i) {
          acc[i] = MFMA32(af[k & 1][i == 3], bf[k & 1][i], acc[i]);
          if (k < 3) { ldb((k + 1) & 1, k + 1, i); if (i < 2) lda((k + 1) & 1, k + 1, i); }
          SBAR();
        }
      }
    };
    const int sr[8] = {0, 0, 1, 0, 0, 1, 1, 1}, si[8] = {0, 1, 0, 2, 3, 1, 2, 3};
    const int sl[8] = {LDS_QB, T_OA, T_O1, LDS_QB, T_OA, T_O1, LDS_QB, T_OA};
    RAWBAR();
    if (wave == 0) STAMP(6);
    issue_o(head_of(0, 1), T_OA);
    unsigned pollv = 0;
#pragma unroll
    for (int s = 0; s < 8; ++s) {
      if (s + 1 < 8) issue_w(head_of(sr[s + 1], si[s + 1]), ((s + 1) & 1) ? T_W1 : T_W0, wave);
      if (s >= 1 && s + 2 < 8) issue_o(head_of(sr[s + 2], si[s + 2]), sl[s + 2]);
      if (s == 0 && wave <= 2) {
        WAIT_VM(8);
        if (lds_add_ret(fl + F_OC + 1, lane) == 2 && lane == 0) gst(gf + FO_O + (b * H + h0 + 16) * 32, tag);
      }
      if (s == 1 && wave == 3) pollv = gld(gf + FO_O + (b * H + head_of(1, lane & 3)) * 32);
      head((s & 1) ? T_W1 : T_W0, sl[s]);
      if (s == 2 && wave == 3) {
        if (!__all(pollv == tag)) gwait(gf + FO_O + (b * H + head_of(1, lane & 3)) * 32, tag);
      }
      if (s + 1 < 8) {
        if (s >= 1 && s + 2 < 8) WAIT_VM(3); else WAIT_VM(0);
        RAWBAR();
      }
    }
    if (wave == 0) STAMP(7);
    RAWBAR();
    {
      LAS char* xch = (LAS char*)lds + wave * 8192 + lane * 16;
#pragma unroll
      for (int i = 0; i < 2; ++i)
#pragma unroll
        for (int j = 0; j < 4; ++j) { const f32x16& t = acc[kh ? i : 2 + i]; f32x4 v = {t[4 * j], t[4 * j + 1], t[4 * j + 2], t[4 * j + 3]}; *(LAS f32x4*)(xch + (i * 4 + j) * 1024) = v; }
      RAWBAR();
      lds_cptr xr = (lds_cptr)lds + (wave ^ 4) * 8192 + lane * 16;
#pragma unroll
      for (int i = 0; i < 2; ++i)
#pragma unroll
        for (int j = 0; j < 4; ++j) { const f32x4 v = *(const LAS f32x4*)(xr + (i * 4 + j) * 1024); f32x16& t = acc[kh ? 2 + i : i];
          t[4 * j] += v[0]; t[4 * j + 1] += v[1]; t[4 * j + 2] += v[2]; t[4 * j + 3] += v[3]; }
    }
    const unsigned pbase = WS_P + (unsigned)((b * 4 + hl) * 4) * (15 * 4096);
#pragma unroll
    for (int ii = 0; ii < 2; ++ii) {
      const int i = 2 * kh + ii, tile = i < 3 ? g * 3 + i : 12 + g;
      if ((i < 3 || g < 3) && (tile & 3) != hg) {
        const f32x16& t = kh ? acc[2 + ii] : acc[ii];
#pragma unroll
        for (int j = 0; j < 4; ++j) {
          f32x4 v = {t[4 * j], t[4 * j + 1], t[4 * j + 2], t[4 * j + 3]};
          __builtin_amdgcn_raw_buffer_store_b128(__builtin_bit_cast(u32x4, v), sows, pbase + (hg * 15 + tile) * 4096 + lane * 16 + j * 1024, 0, 16);
        }
      }
    }
    WAIT_VM(0);
    if (lane == 0) gst(gf + FO_P + (((b * 4 + hl) * 4 + hg) * 8 + wave) * 32, tag);
    gwait(gf + FO_P + (((b * 4 + hl) * 4 + ((hg + 1 + (lane % 3)) & 3)) * 8 + wave) * 32, tag);
#pragma unroll
    for (int ii = 0; ii < 2; ++ii) {
      const int i = 2 * kh + ii, tile = i < 3 ? g * 3 + i : 12 + g;
      if ((i < 3 || g < 3) && (tile & 3) == hg) {
        const f32x16& t = kh ? acc[2 + ii] : acc[ii];
        u32x4 pv[3][4];
#pragma unroll
        for (int o = 0; o < 3; ++o)
#pragma unroll
          for (int j = 0; j < 4; ++j) pv[o][j] = __builtin_amdgcn_raw_buffer_load_b128(sows, lane * 16 + j * 1024, pbase + (((hg + 1 + o) & 3) * 15 + tile) * 4096, 16);
        const int nt = i < 3 ? g : 4, qb = i < 3 ? i : g;
        unsigned short* orow = out + (size_t)(b * SQ + 32 * qb + (lane & 31)) * D + 160 * hl + 32 * nt + 4 * hi;
#pragma unroll
        for (int j = 0; j < 4; ++j) {
          f32x4 v = {t[4 * j], t[4 * j + 1], t[4 * j + 2], t[4 * j + 3]};
#pragma unroll
          for (int o = 0; o < 3; ++o) v += __builtin_bit_cast(f32x4, pv[o][j]);
          u32x2 w2 = {cvtpk(v[0], v[1]), cvtpk(v[2], v[3])};
          *(u32x2*)(orow + 8 * j) = w2;
        }
      }
    }
    if (wave == 0) STAMP(8);
  }
}

extern "C" void kernel_launch(void* const* d_in, const int* in_sizes, int n_in, void* d_out, int out_size, void* d_ws, size_t ws_size, hipStream_t stream) {
  (void)hipFuncSetAttribute((const void*)mha_fused, hipFuncAttributeMaxDynamicSharedMemorySize, LDS_BYTES);
  mha_fused<<<256, 512, LDS_BYTES, stream>>>((const char*)d_in[0], (const char*)d_in[1], (const char*)d_in[2], (const char*)d_in[3], (const char*)d_in[4],
                                              (unsigned short*)d_out, (char*)d_ws);
}
```

```cpp
#include <hip/hip_runtime.h>

typedef short bf16x8 __attribute__((ext_vector_type(8)));
typedef short s16x4 __attribute__((ext_vector_type(4)));
typedef float f32x16 __attribute__((ext_vector_type(16)));
typedef float f32x4 __attribute__((ext_vector_type(4)));
typedef float f32x2 __attribute__((ext_vector_type(2)));
typedef unsigned u32x4 __attribute__((ext_vector_type(4)));
typedef unsigned u32x2 __attribute__((ext_vector_type(2)));
typedef __bf16 bf16x2n __attribute__((ext_vector_type(2)));
#define LAS __attribute__((address_space(3)))
typedef LAS const char* lds_cptr;
typedef volatile LAS unsigned* lds_flag;

constexpr int B = 16, SQ = 96, D = 640, H = 32, SK = 512, DH = 128, HD = H * DH;
constexpr int SLOT = 16384;
constexpr int WSLOT = 45056;
constexpr int LDS_Q0 = 0, LDS_QB = 131072  , LDS_FLAG = 155648, LDS_BYTES = LDS_FLAG + 256;
constexpr int T_W0 = 65536, T_W1 = 0, T_O1 = 106496, T_OA = 40960;
constexpr int F_KVL = 0, F_KVP = 4, F_EP = 8, F_OC = 12, F_OL = 16;
constexpr unsigned WS_P = 16u << 20, WS_P_BYTES = 16 * 4 * 4 * 15 * 4096, WS_F = 40u << 20;
constexpr unsigned FO_EPOCH = 0, FO_SEEN = 64, FO_O = FO_SEEN + 256 * 16, FO_P = FO_O + 512 * 32;
constexpr float CS = 0.088388347648318447f * 1.4426950408889634f;
constexpr float THR = 8.0f;


#define MFMA32(a, b, c) __builtin_amdgcn_mfma_f32_32x32x16_bf16(a, b, c, 0, 0, 0)
#define SBAR() __builtin_amdgcn_sched_barrier(0)
#define MEMBAR() asm volatile("" ::: "memory")
#define PIN(x) asm volatile("" : "+v"(x))
#define DI __device__ __forceinline__
#define WAIT_VM(n) asm volatile("s_waitcnt vmcnt(" #n ")" ::: "memory")
#define WAIT_LGKM0() asm volatile("s_waitcnt lgkmcnt(0)" ::: "memory")
#define RAWBAR() do { WAIT_LGKM0(); __builtin_amdgcn_s_barrier(); MEMBAR(); } while (0)

DI unsigned cvtpk(float lo, float hi) { f32x2 v = {lo, hi}; return __builtin_bit_cast(unsigned, __builtin_convertvector(v, bf16x2n)); }
DI s16x4 trd(lds_cptr p) { return __builtin_amdgcn_ds_read_tr16_b64_v4i16((LAS s16x4*)p); }
DI bf16x8 cat(s16x4 a, s16x4 b) { return __builtin_shufflevector(a, b, 0, 1, 2, 3, 4, 5, 6, 7); }
template <bool SCALE> DI bf16x8 pack8(const f32x16& x, int s) {
  const float c = SCALE ? CS : 1.0f;
  u32x4 w = {cvtpk(x[8 * s] * c, x[8 * s + 1] * c), cvtpk(x[8 * s + 2] * c, x[8 * s + 3] * c), cvtpk(x[8 * s + 4] * c, x[8 * s + 5] * c), cvtpk(x[8 * s + 6] * c, x[8 * s + 7] * c)};
  return __builtin_bit_cast(bf16x8, w);
}
DI unsigned rfl(unsigned v) { return __builtin_amdgcn_readfirstlane(v); }
DI unsigned lds_addr(lds_flag f) { return (unsigned)(uintptr_t)f; }

DI void wait1(unsigned early, lds_flag f, unsigned need) {
  unsigned t, sv, cnt;
  asm volatile("s_cmp_ge_u32 %3, %4\n\ts_cbranch_scc1 W1E%=\n\ts_mov_b32 %2, 0\n"
               "W1L%=:\n\tds_read_b32 %0, %5\n\ts_waitcnt lgkmcnt(0)\n\tv_readfirstlane_b32 %1, %0\n\ts_cmp_ge_u32 %1, %4\n\ts_cbranch_scc1 W1E%=\n\t"
               "s_sleep 1\n\ts_add_u32 %2, %2, 1\n\ts_cmp_lt_u32 %2, 0x40000\n\ts_cbranch_scc1 W1L%=\n"
               "W1E%=:"
               : "=&v"(t), "=&s"(sv), "=&s"(cnt) : "s"(early), "s"(need), "v"(lds_addr(f)) : "memory", "scc");
}
DI void wait4(lds_flag f, unsigned need) {
  unsigned a0, a1, a2, m, sv, cnt;
  asm volatile("s_mov_b32 %5, 0\n"
               "W4L%=:\n\tds_read_b32 %3, %7\n\tds_read_b32 %0, %7 offset:4\n\tds_read_b32 %1, %7 offset:8\n\tds_read_b32 %2, %7 offset:12\n\ts_waitcnt lgkmcnt(0)\n\t"
               "v_min_u32 %3, %3, %0\n\tv_min3_u32 %3, %3, %1, %2\n\ts_nop 0\n\tv_readfirstlane_b32 %4, %3\n\ts_cmp_ge_u32 %4, %6\n\ts_cbranch_scc1 W4E%=\n\t"
               "s_sleep 1\n\ts_add_u32 %5, %5, 1\n\ts_cmp_lt_u32 %5, 0x40000\n\ts_cbranch_scc1 W4L%=\n"
               "W4E%=:"
               : "=&v"(a0), "=&v"(a1), "=&v"(a2), "=&v"(m), "=&s"(sv), "=&s"(cnt) : "s"(need), "v"(lds_addr(f)) : "memory", "scc");
}
DI unsigned min4(lds_flag f) { u32x4 v = *(volatile LAS u32x4*)f; unsigned m = v[0] < v[1] ? v[0] : v[1]; m = m < v[2] ? m : v[2]; return rfl(m < v[3] ? m : v[3]); }
DI unsigned lds_add_ret(lds_flag f, int lane) {
  unsigned old = 0;
  if (lane == 0) old = __hip_atomic_fetch_add((LAS unsigned*)f, 1u, __ATOMIC_RELAXED, __HIP_MEMORY_SCOPE_WORKGROUP);
  return rfl(old);
}
DI unsigned gld(const unsigned* p) { return __hip_atomic_load(p, __ATOMIC_RELAXED, __HIP_MEMORY_SCOPE_AGENT); }
DI void gst(unsigned* p, unsigned v) { __hip_atomic_store(p, v, __ATOMIC_RELAXED, __HIP_MEMORY_SCOPE_AGENT); }
DI void gwait(const unsigned* p, unsigned tag) {
  for (unsigned spins = 0; spins < (1u << 15); ++spins) {
    if (__all(gld(p) == tag)) break;
    __builtin_amdgcn_s_sleep(2);
  }
  MEMBAR();
}

#define DMA_HEAD "s_nop 2\n\ts_mov_b32 m0, %0\n\ts_nop 0\n\t"
#define DMA_BODY(POL) \
  if constexpr (N == 1) asm volatile(DMA_HEAD "buffer_load_dwordx4 %1, %2, %3 offen" POL " lds" ::"s"(dst), "v"(va), "s"(srd), "s"(soff) : "m0", "memory"); \
  else if constexpr (N == 2) asm volatile(DMA_HEAD "buffer_load_dwordx4 %1, %3, %4 offen" POL " lds\n\tbuffer_load_dwordx4 %2, %3, %4 offen offset:1024" POL " lds" \
                                          ::"s"(dst), "v"(va), "v"(vb), "s"(srd), "s"(soff) : "m0", "memory"); \
  else if constexpr (N == 3) asm volatile(DMA_HEAD "buffer_load_dwordx4 %1, %4, %5 offen" POL " lds\n\tbuffer_load_dwordx4 %2, %4, %5 offen offset:1024" POL " lds\n\t" \
                                          "buffer_load_dwordx4 %3, %4, %5 offen offset:2048" POL " lds" ::"s"(dst), "v"(va), "v"(vb), "v"(vc), "s"(srd), "s"(soff) : "m0", "memory"); \
  else asm volatile(DMA_HEAD "buffer_load_dwordx4 %1, %5, %6 offen" POL " lds\n\tbuffer_load_dwordx4 %2, %5, %6 offen offset:1024" POL " lds\n\t" \
                    "buffer_load_dwordx4 %3, %5, %6 offen offset:2048" POL " lds\n\tbuffer_load_dwordx4 %4, %5, %6 offen offset:3072" POL " lds" \
                    ::"s"(dst), "v"(va), "v"(vb), "v"(vc), "v"(vd), "s"(srd), "s"(soff) : "m0", "memory");
template <int N, int POL>
DI void dma(__amdgpu_buffer_rsrc_t srd, unsigned soff, unsigned dst, unsigned va, unsigned vb = 0, unsigned vc = 0, unsigned vd = 0) {
  dst = rfl(dst); soff = rfl(soff);
  if constexpr (POL == 0) { DMA_BODY("") } else if constexpr (POL == 1) { DMA_BODY(" nt") } else { DMA_BODY(" sc1") }
}
template <int PITCH> DI unsigned dma_voff(int i) { return (i >> 4) * PITCH + 16 * ((i & 15) ^ ((i >> 4) << 2)); }

DI int wslot(int t) { return (t % 3) * WSLOT; }
DI void qproj(lds_cptr lds, int i, int lane, f32x16 (&acc)[2][3]) {
  const int hi = lane >> 5, blk = (lane >> 4) & 1, q = (lane & 15) >> 2, pp = lane & 3, r32 = lane & 31;
  lds_cptr aw[4], ax[4];
#pragma unroll
  for (int d = 0; d < 2; ++d)
#pragma unroll
    for (int t = 0; t < 2; ++t)
      aw[2 * d + t] = lds + 16384 * (i >> 1) + 2048 * hi + 1024 * t + 256 * q + 16 * (4 * ((2 * (i & 1) + d) ^ q) + ((2 * blk + (pp & 1)) ^ (2 * hi + t))) + 8 * (pp >> 1);
#pragma unroll
  for (int u = 0; u < 4; ++u) ax[u] = lds + 32768 + 128 * r32 + 16 * ((2 * u + hi) ^ ((r32 >> 1) & 7));
  bf16x8 wf[2][2], xf[2][3];
  auto ldw = [&](int buf, int so, int u, int d) { wf[buf][d] = cat(trd(aw[2 * d] + so + 4096 * u), trd(aw[2 * d + 1] + so + 4096 * u)); };
  auto ldx = [&](int buf, int so, int u, int qb) { xf[buf][qb] = *(const LAS bf16x8*)(ax[u] + so + 4096 * qb); };
  RAWBAR();
  int so = wslot(0);
#pragma unroll
  for (int d = 0; d < 2; ++d) ldw(0, so, 0, d);
#pragma unroll
  for (int qb = 0; qb < 3; ++qb) ldx(0, so, 0, qb);
#pragma unroll 1
  for (int t = 0; t < 10; ++t) {
    const int son = wslot(t + 1);
#pragma unroll
    for (int u = 0; u < 4; ++u) {
      SBAR();
      const int nb = (u + 1) & 1, nso = u < 3 ? so : son, nu = (u + 1) & 3;
#pragma unroll
      for (int g = 0; g < 6; ++g) {
        const int d = g / 3, qb = g % 3;
        acc[d][qb] = MFMA32(wf[u & 1][d], xf[u & 1][qb], acc[d][qb]);
        if (u < 3) {
          if (g == 0) ldw(nb, nso, nu, 0);
          else if (g < 4) ldx(nb, nso, nu, g - 1);
          else if (g == 4) ldw(nb, nso, nu, 1);
        } else {
          if (g == 2) { SBAR(); RAWBAR(); ldw(nb, nso, nu, 0); ldx(nb, nso, nu, 0); }
          else if (g == 3) { ldx(nb, nso, nu, 1); ldx(nb, nso, nu, 2); }
          else if (g == 4) ldw(nb, nso, nu, 1);
        }
        SBAR();
      }
    }
    so = son;
  }
}

struct AAddr { lds_cptr k[8]; lds_cptr v[8]; };
DI void aaddr(AAddr& a, lds_cptr kv, int lane) {
  const int r32 = lane & 31, hi = lane >> 5, blk = (lane >> 4) & 1, q = (lane & 15) >> 2, p = lane & 3;
  const int f = ((r32 & 3) << 2) | ((r32 >> 2) & 3);
#pragma unroll
  for (int s = 0; s < 8; ++s) a.k[s] = kv + 256 * r32 + 16 * ((2 * s + hi) ^ f);
#pragma unroll
  for (int c = 0; c < 4; ++c)
#pragma unroll
    for (int t = 0; t < 2; ++t)
      a.v[2 * c + t] = kv + 2 * SLOT + 2048 * t + 1024 * hi + 256 * q + 16 * (4 * (c ^ q) + ((2 * blk + (p >> 1)) ^ (2 * t + hi))) + 8 * (p & 1);
}
DI float max3(float a, float b, float c) { return __builtin_fmaxf(__builtin_fmaxf(a, b), c); }
DI void attn_head(AAddr& a, lds_flag fl, int n0  , lds_flag prog, int lane, bf16x8 (&qf)[8], lds_cptr qnext,
                  __amdgpu_buffer_rsrc_t so, unsigned ooff, unsigned* pend_flag, unsigned tag, LAS char* olds  , unsigned olds_need, lds_flag olds_cnt) {
  f32x16 o[4] = {};
  f32x16 ol = {};
  f32x16 negr = {};
  const bf16x8 ones = {0x3F80, 0x3F80, 0x3F80, 0x3F80, 0x3F80, 0x3F80, 0x3F80, 0x3F80};
  bf16x8 fr[2][4];
  auto ldk = [&](int step, int i) {
    const int tt = step >> 2, kb = (step >> 1) & 1, hf = step & 1;
    fr[step & 1][i] = *(const LAS bf16x8*)(a.k[4 * hf + i] + SLOT * tt + 8192 * kb);
  };
  auto ldv = [&](int step, int c) {
    const int tt = step >> 2, ks = step & 3;
    fr[step & 1][c] = cat(trd(a.v[2 * c] + SLOT * tt + 4096 * ks), trd(a.v[2 * c + 1] + SLOT * tt + 4096 * ks));
  };
  wait1(0, fl + F_KVL, n0 + 1);
#pragma unroll
  for (int i = 0; i < 4; ++i) ldk(0, i);
#pragma unroll 1
  for (int ch = 0; ch < 4; ++ch) {
    const int n = n0 + 8 * ch;
    f32x16 s[4];
    unsigned early = 0;
    float mx = -__builtin_inff();
#pragma unroll
    for (int step = 0; step < 8; ++step) {
      if (step & 1) { MEMBAR(); prog[0] = n + (step >> 1) + 1; if (step < 7) wait1(rfl(early), fl + F_KVL, n + (step >> 1) + 2); }
      SBAR();
      const int ti = step >> 1, hf = step & 1;
#pragma unroll
      for (int i = 0; i < 4; ++i) {
        if (hf == 0 && i == 0) s[ti] = MFMA32(fr[step & 1][0], qf[0], negr);
        else s[ti] = MFMA32(fr[step & 1][i], qf[4 * hf + i], s[ti]);
        if (step + 1 < 8) ldk(step + 1, i);
        if ((!(step & 1) || step == 7) && i == 0) early = fl[F_KVL];
        if (ti >= 1 && (hf == 1 || i >= 2)) {
          const int g = hf == 0 ? i - 2 : 2 + i;
          const f32x16& x = s[ti - 1];
          if (g < 2) { mx = max3(mx, x[4 * g], x[4 * g + 1]); mx = max3(mx, x[4 * g + 2], x[4 * g + 3]); }
          else mx = max3(mx, x[8 + 2 * (g - 2)], x[9 + 2 * (g - 2)]);
          PIN(mx);
        }
        SBAR();
      }
    }
#pragma unroll
    for (int i = 0; i < 8; ++i) a.k[i] += (ch & 1) ? 65536 : -65536;
    wait1(rfl(early), fl + F_KVL, n + 5);
#pragma unroll
    for (int c = 0; c < 4; ++c) ldv(0, c);
    SBAR();
#pragma unroll
    for (int r = 0; r < 16; r += 2) mx = max3(mx, s[3][r], s[3][r + 1]);
    { auto rr = __builtin_amdgcn_permlane32_swap(__float_as_uint(mx), __float_as_uint(mx), false, false);
      mx = __builtin_fmaxf(__uint_as_float(rr[0]), __uint_as_float(rr[1])); }
    if (ch == 0 || __any(mx > THR)) {
      const float dl = ch == 0 ? mx : __builtin_fmaxf(mx, 0.f);
#pragma unroll
      for (int i = 0; i < 4; ++i)
#pragma unroll
        for (int r = 0; r < 16; ++r) s[i][r] -= dl;
#pragma unroll
      for (int r = 0; r < 16; ++r) negr[r] -= dl;
      if (ch > 0) {
        const float alpha = __builtin_amdgcn_exp2f(-dl);
        ol[0] *= alpha;
#pragma unroll
        for (int c = 0; c < 4; ++c)
#pragma unroll
          for (int r = 0; r < 16; ++r) o[c][r] *= alpha;
      }
    }
    unsigned pw[3][4];
    float pe[2][2];
    auto expo = [&](int step, int i) {
      const f32x16& x = s[step >> 1];
      const int e = 8 * (step & 1) + 2 * i;
      pe[i & 1][0] = __builtin_amdgcn_exp2f(x[e]); pe[i & 1][1] = __builtin_amdgcn_exp2f(x[e + 1]);
      PIN(pe[i & 1][0]); PIN(pe[i & 1][1]);
    };
    auto packw = [&](int step, int i) { pw[step % 3][i] = cvtpk(pe[i & 1][0], pe[i & 1][1]); PIN(pw[step % 3][i]); };
#pragma unroll
    for (int st = 0; st < 2; ++st)
#pragma unroll
      for (int i = 0; i < 4; ++i) { expo(st, i); packw(st, i); }
    SBAR();
#pragma unroll
    for (int step = 0; step < 8; ++step) {
      if (step & 1) { MEMBAR(); prog[0] = n + 4 + (step >> 1) + 1; wait1(rfl(early), fl + F_KVL, (step < 7 || ch < 3) ? n + 4 + (step >> 1) + 2 : 0); }
      SBAR();
      u32x4 pv = {pw[step % 3][0], pw[step % 3][1], pw[step % 3][2], pw[step % 3][3]};
      const bf16x8 pf = __builtin_bit_cast(bf16x8, pv);
#pragma unroll
      for (int c = 0; c < 4; ++c) {
        o[c] = MFMA32(fr[step & 1][c], pf, o[c]);
        if (step + 1 < 8) ldv(step + 1, c);
        else ldk(0, c);
        if (step + 2 < 8) { if (c > 0) packw(step + 2, c - 1); expo(step + 2, c); }
        if (!(step & 1) && c == 0) early = fl[F_KVL];
        SBAR();
      }
      ol = MFMA32(ones, pf, ol);
      if (step + 2 < 8) packw(step + 2, 3);
      SBAR();
    }
#pragma unroll
    for (int i = 0; i < 8; ++i) a.v[i] += (ch & 1) ? 65536 : -65536;
    if (ch == 0 && pend_flag) {
      WAIT_VM(0);
      if (lds_add_ret(fl + F_OC, lane) == 2 && lane == 0) gst(pend_flag, tag);
    }
  }
  const float inv = 1.0f / ol[0];
  wait4(fl + F_KVP, olds_need);
  if (qnext) {
#pragma unroll
    for (int s = 0; s < 8; ++s) qf[s] = *(const LAS bf16x8*)(qnext + s * 1024 + lane * 16);
#pragma unroll
    for (int s = 0; s < 8; ++s) PIN(qf[s]);
    WAIT_LGKM0();
  }
#pragma unroll
  for (int c = 0; c < 4; ++c) {
#pragma unroll
    for (int r = 0; r < 16; ++r) o[c][r] *= inv;
    const bf16x8 f0 = pack8<false>(o[c], 0), f1 = pack8<false>(o[c], 1);
    *(LAS bf16x8*)(olds + (2 * c) * 1024 + lane * 16) = f0;
    *(LAS bf16x8*)(olds + (2 * c + 1) * 1024 + lane * 16) = f1;
    __builtin_amdgcn_raw_buffer_store_b128(__builtin_bit_cast(u32x4, f0), so, ooff + lane * 16 + (2 * c) * 1024, 0, 16);
    __builtin_amdgcn_raw_buffer_store_b128(__builtin_bit_cast(u32x4, f1), so, ooff + lane * 16 + (2 * c + 1) * 1024, 0, 16);
  }
  WAIT_LGKM0();
  lds_add_ret(olds_cnt, lane);
}

__global__ __launch_bounds__(512, 2) void mha_fused(const char* __restrict__ x, const char* __restrict__ kc, const char* __restrict__ vc, const char* __restrict__ wq,
                                                     const char* __restrict__ wo, unsigned short* __restrict__ out, char* __restrict__ ws) {
  extern __shared__ __attribute__((aligned(16))) char lds[];
  const int tid = threadIdx.x, lane = tid & 63, wave = rfl(tid >> 6);
  const int bid = blockIdx.x, xcd = bid & 7, idx = bid >> 3;
  const int hg = xcd >> 1, hl = idx & 3;
  const int b = 8 * (xcd & 1) + (idx >> 2), h0 = 4 * hg + hl;
  unsigned* gf = (unsigned*)(ws + WS_F);
  lds_flag fl = (lds_flag)(lds + LDS_FLAG);
  const unsigned lds0 = (unsigned)(uintptr_t)(LAS char*)lds;

  const __amdgpu_buffer_rsrc_t sows = __builtin_amdgcn_make_buffer_rsrc((void*)ws, 0, WS_P + WS_P_BYTES, 0x00020000);
  const __amdgpu_buffer_rsrc_t swo = __builtin_amdgcn_make_buffer_rsrc((void*)wo, 0, HD * D * 2, 0x00020000);
  auto head_of = [&](int r, int i) { return 16 * r + 4 * hg + ((i + hl) & 3); };
  unsigned wv[5];
#pragma unroll
  for (int j = 0; j < 5; ++j) { const int pi = 64 * j + lane; wv[j] = (pi / 20) * 1280 + (pi % 20) * 16 - (j < 4 ? 1024 * j : 0); }
  auto issue_w = [&](int h, int buf, int part) {
    const unsigned sb = (unsigned)(h * 128 + 16 * part) * 1280u + hl * 320u, dst = lds0 + buf + part * 5120;
    dma<4, 0>(swo, sb, dst, wv[0], wv[1], wv[2], wv[3]); dma<1, 0>(swo, sb, dst + 4096, wv[4]);
  };

  if (wave == 7) {
    __builtin_amdgcn_s_setprio(3);
    const __amdgpu_buffer_rsrc_t sk = __builtin_amdgcn_make_buffer_rsrc((void*)kc, 0, B * H * SK * DH * 2, 0x00020000);
    const __amdgpu_buffer_rsrc_t sv = __builtin_amdgcn_make_buffer_rsrc((void*)vc, 0, B * H * SK * DH * 2, 0x00020000);
    const unsigned v0 = dma_voff<256>(lane);
    auto kvunit = [&](int u) {
      const int c = u >> 3, head = h0 + 16 * (c >> 2), isv = (u >> 2) & 1;
      const unsigned sbase = (unsigned)((b * H + head) * SK + (c & 3) * 128 + (u & 3) * 32) * 256u;
      const unsigned dst = lds0 + ((c & 1) ? 0 : 65536) + (u & 7) * 8192;
      dma<4, 1>(isv ? sv : sk, sbase, dst, v0, v0 ^ 16, v0 ^ 32, v0 ^ 48);
      dma<4, 1>(isv ? sv : sk, sbase + 4096, dst + 4096, v0, v0 ^ 16, v0 ^ 32, v0 ^ 48);
    };
#pragma unroll 1
    for (int t = 0; t < 10; ++t) RAWBAR();
    unsigned freed = 0;
    int P = 0;
#pragma unroll 1
    for (int u = 0; u < 64; ++u) {
      const unsigned need = u < 8 ? 0 : u < 16 ? 1 : u - 15;
      if (freed < need) {
        freed = min4(fl + F_KVP);
        if (freed < need) {
          if (P < u) { WAIT_VM(0); P = u; fl[F_KVL] = P; }
          wait4(fl + F_KVP, need);
          freed = need;
        }
      }
      kvunit(u);
      if (u == 1 || u == 3) RAWBAR();
      WAIT_VM(24);
      if (P < u - 2) { P = u - 2; fl[F_KVL] = P; }
    }
    WAIT_VM(8); fl[F_KVL] = 63;
    WAIT_VM(0); fl[F_KVL] = 64;
    __builtin_amdgcn_s_setprio(0);
  } else if (wave <= 3) {
    f32x16 acc[2][3] = {};
    qproj((lds_cptr)lds, wave, lane, acc);
#pragma unroll
    for (int d = 0; d < 2; ++d)
#pragma unroll
      for (int qb = 0; qb < 3; ++qb) {
        LAS char* qx = (LAS char*)lds + ((wave >> 1) ? LDS_QB : LDS_Q0) + qb * 8192 + (2 * (2 * (wave & 1) + d)) * 1024 + lane * 16;
        *(LAS bf16x8*)qx = pack8<true>(acc[d][qb], 0);
        *(LAS bf16x8*)(qx + 1024) = pack8<true>(acc[d][qb], 1);
      }
    RAWBAR();
  } else if (wave == 4) {
    fl[lane] = (lane == F_KVP + 3) ? 0x7fffffffu : 0u;
    const unsigned tag0 = gld(gf + FO_EPOCH) + 1u;
    fl[F_EP] = tag0;
    if (lane == 0) gst(gf + FO_SEEN + bid * 16, tag0);
#pragma unroll 1
    for (int t = 0; t < 9; ++t) RAWBAR();
    unsigned wrm = 0;
#pragma unroll
    for (int i = 0; i < 5; ++i) {
      const int L = idx * 320 + i * 64 + lane, hh = L / 1280;
      wrm ^= *(const unsigned*)(wo + (size_t)(16 * (hh >> 2) + 4 * hg + (hh & 3)) * 163840 + (size_t)(L - hh * 1280) * 128);
    }
#pragma unroll 1
    for (int t = 9; t < 12; ++t) RAWBAR();
    asm volatile("" :: "v"(wrm));
    if (bid == 0) {
#pragma unroll
      for (int i = 0; i < 4; ++i) gwait(gf + FO_SEEN + (i * 64 + lane) * 16, tag0);
      if (lane == 0) gst(gf + FO_EPOCH, tag0);
    }
  } else {
    __builtin_amdgcn_s_setprio(3);
    const int pt = wave - 5;
    const int c0 = ((b & 7) + 2 * hl) % 10;
    const __amdgpu_buffer_rsrc_t sw = __builtin_amdgcn_make_buffer_rsrc((void*)wq, 0, D * HD * 2, 0x00020000);
    const __amdgpu_buffer_rsrc_t sx = __builtin_amdgcn_make_buffer_rsrc((void*)x, 0, B * SQ * D * 2, 0x00020000);
    const unsigned v0 = dma_voff<8192>(lane);
    const unsigned xv0 = (lane >> 3) * 1280 + (((lane & 7) ^ (lane >> 4)) * 16);
    auto issue = [&](int t) {
      const int cc = t + c0, c = cc >= 10 ? cc - 10 : cc;
      const unsigned sbase = (unsigned)(c * 64) * 8192u + (h0 + 16 * pt) * 256u;
      const unsigned dst = lds0 + wslot(t);
      const unsigned xs = (unsigned)(b * SQ + 48 * pt) * 1280u + c * 128u;
      dma<4, 0>(sx, xs, dst + 32768 + 6 * pt * 1024, xv0, (xv0 ^ 64) + 9216, xv0 + 2 * 9216, (xv0 ^ 64) + 3 * 9216);
      dma<2, 0>(sx, xs + 4 * 10240, dst + 32768 + (6 * pt + 4) * 1024, xv0, (xv0 ^ 64) + 9216);
#pragma unroll
      for (int j = 0; j < 4; ++j)
        dma<4, 0>(sw, sbase + j * 4 * 32768, dst + pt * 16384 + j * 4096, v0, (v0 ^ 16) + 31744, (v0 ^ 32) + 2 * 31744, (v0 ^ 48) + 3 * 31744);
    };
    issue(0); issue(1);
    WAIT_VM(22); issue(2); RAWBAR();
#pragma unroll 1
    for (int t = 0; t < 10; ++t) {
      if (t < 8) WAIT_VM(22); else WAIT_VM(0);
      RAWBAR();
      if (t + 3 < 10) issue(t + 3);
    }
    RAWBAR();
    __builtin_amdgcn_s_setprio(0);
  }
  const unsigned tag = rfl(fl[F_EP]);

  if (wave <= 2) {
    __builtin_amdgcn_s_setprio(1);
    const int qb = wave;
    bf16x8 qf[8];
    AAddr aa; aaddr(aa, (lds_cptr)lds + 65536, lane);
    {
      lds_cptr qx = (lds_cptr)lds + LDS_Q0 + qb * 8192;
#pragma unroll
      for (int s = 0; s < 8; ++s) qf[s] = *(const LAS bf16x8*)(qx + s * 1024 + lane * 16);
#pragma unroll
      for (int s = 0; s < 8; ++s) PIN(qf[s]);
      WAIT_LGKM0();
    }
#pragma unroll 1
    for (int r = 0; r < 2; ++r) {
      attn_head(aa, fl, 32 * r, fl + F_KVP + qb, lane, qf, r == 0 ? (lds_cptr)lds + LDS_QB + qb * 8192 : (lds_cptr) nullptr,
                sows, (unsigned)(((b * H + h0 + 16 * r) * 3 + qb) * 8192), r == 1 ? gf + FO_O + (b * H + h0) * 32 : nullptr, tag,
                (LAS char*)lds + (r ? T_O1 : LDS_QB) + qb * 8192, r ? 56u : 0u, fl + F_OL + r);
    }
    __builtin_amdgcn_s_setprio(0);
  } else {
    if (wave == 3) {
      wait1(0, fl + F_OL, 3);
      gwait(gf + FO_O + (b * H + head_of(0, lane & 3)) * 32, tag);
    } else if (wave < 7) {
      wait4(fl + F_KVP, 56);
#pragma unroll
      for (int p = 0; p < 3; ++p) if (3 * p + (wave - 4) < 8) issue_w(head_of(0, 0), T_W0, 3 * p + (wave - 4));
      WAIT_VM(0);
    }
  }

  {
    const int g = wave & 3, kh = wave >> 2;
    const int hi = lane >> 5, blk = (lane >> 4) & 1, q = (lane & 15) >> 2, p = lane & 3;
    const int aoff = 320 * (64 * kh + 4 * hi + q) + 32 * blk + 8 * p + 64 * g;
    const int gx = g < 3 ? g : 0;
    f32x16 acc[4] = {};
    auto issue_o = [&](int h, int slot) {
      dma<3, 2>(sows, (unsigned)((b * H + h) * 24576 + wave * 3072), lds0 + slot + wave * 3072, lane * 16, lane * 16, lane * 16);
    };
    auto head = [&](int wbuf, int oslot) {
      lds_cptr a0 = (lds_cptr)lds + wbuf + aoff, a1 = a0 + 64 * (4 - g);
      lds_cptr ob = (lds_cptr)lds + oslot + kh * 4096 + lane * 16;
      bf16x8 af[2][2], bf[2][4];
      auto lda = [&](int buf, int k, int i) { lds_cptr o = (i ? a1 : a0) + 320 * 16 * k; af[buf][i] = cat(trd(o), trd(o + 320 * 8)); };
      auto ldb = [&](int buf, int k, int i) { bf[buf][i] = *(const LAS bf16x8*)(ob + (i < 3 ? i : gx) * 8192 + k * 1024); };
      lda(0, 0, 0); ldb(0, 0, 0); ldb(0, 0, 1); ldb(0, 0, 2); lda(0, 0, 1); ldb(0, 0, 3);
#pragma unroll
      for (int k = 0; k < 4; ++k) {
        SBAR();
#pragma unroll
        for (int i = 0; i < 4; ++i) {
          acc[i] = MFMA32(af[k & 1][i == 3], bf[k & 1][i], acc[i]);
          if (k < 3) { ldb((k + 1) & 1, k + 1, i); if (i < 2) lda((k + 1) & 1, k + 1, i); }
          SBAR();
        }
      }
    };
    const int sr[8] = {0, 0, 1, 0, 0, 1, 1, 1}, si[8] = {0, 1, 0, 2, 3, 1, 2, 3};
    const int sl[8] = {LDS_QB, T_OA, T_O1, LDS_QB, T_OA, T_O1, LDS_QB, T_OA};
    RAWBAR();
    issue_o(head_of(0, 1), T_OA);
    unsigned pollv = 0;
#pragma unroll
    for (int s = 0; s < 8; ++s) {
      if (s + 1 < 8) issue_w(head_of(sr[s + 1], si[s + 1]), ((s + 1) & 1) ? T_W1 : T_W0, wave);
      if (s >= 1 && s + 2 < 8) issue_o(head_of(sr[s + 2], si[s + 2]), sl[s + 2]);
      if (s == 0 && wave <= 2) {
        WAIT_VM(8);
        if (lds_add_ret(fl + F_OC + 1, lane) == 2 && lane == 0) gst(gf + FO_O + (b * H + h0 + 16) * 32, tag);
      }
      if (s == 1 && wave == 3) pollv = gld(gf + FO_O + (b * H + head_of(1, lane & 3)) * 32);
      head((s & 1) ? T_W1 : T_W0, sl[s]);
      if (s == 2 && wave == 3) {
        if (!__all(pollv == tag)) gwait(gf + FO_O + (b * H + head_of(1, lane & 3)) * 32, tag);
      }
      if (s + 1 < 8) {
        if (s >= 1 && s + 2 < 8) WAIT_VM(3); else WAIT_VM(0);
        RAWBAR();
      }
    }
    RAWBAR();
    {
      LAS char* xch = (LAS char*)lds + wave * 8192 + lane * 16;
#pragma unroll
      for (int i = 0; i < 2; ++i)
#pragma unroll
        for (int j = 0; j < 4; ++j) { const f32x16& t = acc[kh ? i : 2 + i]; f32x4 v = {t[4 * j], t[4 * j + 1], t[4 * j + 2], t[4 * j + 3]}; *(LAS f32x4*)(xch + (i * 4 + j) * 1024) = v; }
      RAWBAR();
      lds_cptr xr = (lds_cptr)lds + (wave ^ 4) * 8192 + lane * 16;
#pragma unroll
      for (int i = 0; i < 2; ++i)
#pragma unroll
        for (int j = 0; j < 4; ++j) { const f32x4 v = *(const LAS f32x4*)(xr + (i * 4 + j) * 1024); f32x16& t = acc[kh ? 2 + i : i];
          t[4 * j] += v[0]; t[4 * j + 1] += v[1]; t[4 * j + 2] += v[2]; t[4 * j + 3] += v[3]; }
    }
    const unsigned pbase = WS_P + (unsigned)((b * 4 + hl) * 4) * (15 * 4096);
#pragma unroll
    for (int ii = 0; ii < 2; ++ii) {
      const int i = 2 * kh + ii, tile = i < 3 ? g * 3 + i : 12 + g;
      if ((i < 3 || g < 3) && (tile & 3) != hg) {
        const f32x16& t = kh ? acc[2 + ii] : acc[ii];
#pragma unroll
        for (int j = 0; j < 4; ++j) {
          f32x4 v = {t[4 * j], t[4 * j + 1], t[4 * j + 2], t[4 * j + 3]};
          __builtin_amdgcn_raw_buffer_store_b128(__builtin_bit_cast(u32x4, v), sows, pbase + (hg * 15 + tile) * 4096 + lane * 16 + j * 1024, 0, 16);
        }
      }
    }
    WAIT_VM(0);
    if (lane == 0) gst(gf + FO_P + (((b * 4 + hl) * 4 + hg) * 8 + wave) * 32, tag);
    gwait(gf + FO_P + (((b * 4 + hl) * 4 + ((hg + 1 + (lane % 3)) & 3)) * 8 + wave) * 32, tag);
#pragma unroll
    for (int ii = 0; ii < 2; ++ii) {
      const int i = 2 * kh + ii, tile = i < 3 ? g * 3 + i : 12 + g;
      if ((i < 3 || g < 3) && (tile & 3) == hg) {
        const f32x16& t = kh ? acc[2 + ii] : acc[ii];
        u32x4 pv[3][4];
#pragma unroll
        for (int o = 0; o < 3; ++o)
#pragma unroll
          for (int j = 0; j < 4; ++j) pv[o][j] = __builtin_amdgcn_raw_buffer_load_b128(sows, lane * 16 + j * 1024, pbase + (((hg + 1 + o) & 3) * 15 + tile) * 4096, 16);
        const int nt = i < 3 ? g : 4, qb = i < 3 ? i : g;
        unsigned short* orow = out + (size_t)(b * SQ + 32 * qb + (lane & 31)) * D + 160 * hl + 32 * nt + 4 * hi;
#pragma unroll
        for (int j = 0; j < 4; ++j) {
          f32x4 v = {t[4 * j], t[4 * j + 1], t[4 * j + 2], t[4 * j + 3]};
#pragma unroll
          for (int o = 0; o < 3; ++o) v += __builtin_bit_cast(f32x4, pv[o][j]);
          u32x2 w2 = {cvtpk(v[0], v[1]), cvtpk(v[2], v[3])};
          *(u32x2*)(orow + 8 * j) = w2;
        }
      }
    }
  }
}

extern "C" void kernel_launch(void* const* d_in, const int* in_sizes, int n_in, void* d_out, int out_size, void* d_ws, size_t ws_size, hipStream_t stream) {
  (void)hipFuncSetAttribute((const void*)mha_fused, hipFuncAttributeMaxDynamicSharedMemorySize, LDS_BYTES);
  mha_fused<<<256, 512, LDS_BYTES, stream>>>((const char*)d_in[0], (const char*)d_in[1], (const char*)d_in[2], (const char*)d_in[3], (const char*)d_in[4],
                                              (unsigned short*)d_out, (char*)d_ws);
}
```

```cpp
#include <hip/hip_runtime.h>

typedef short bf16x8 __attribute__((ext_vector_type(8)));
typedef short s16x4 __attribute__((ext_vector_type(4)));
typedef float f32x16 __attribute__((ext_vector_type(16)));
typedef float f32x4 __attribute__((ext_vector_type(4)));
typedef float f32x2 __attribute__((ext_vector_type(2)));
typedef unsigned u32x4 __attribute__((ext_vector_type(4)));
typedef unsigned u32x2 __attribute__((ext_vector_type(2)));
typedef __bf16 bf16x2n __attribute__((ext_vector_type(2)));
#define LAS __attribute__((address_space(3)))
typedef LAS const char* lds_cptr;
typedef volatile LAS unsigned* lds_flag;

constexpr int B = 16, SQ = 96, D = 640, H = 32, SK = 512, DH = 128, HD = H * DH;
constexpr int SLOT = 16384;
constexpr int WSLOT = 45056;
constexpr int LDS_Q0 = 0, LDS_QB = 131072  , LDS_FLAG = 155648, LDS_BYTES = LDS_FLAG + 256;
constexpr int T_W0 = 65536, T_W1 = 0, T_O1 = 106496, T_OA = 40960;
constexpr int F_KVL = 0, F_KVP = 4, F_EP = 8, F_OC = 12, F_OL = 16;
constexpr unsigned WS_P = 16u << 20, WS_P_BYTES = 16 * 4 * 4 * 15 * 4096, WS_F = 40u << 20;
constexpr unsigned FO_EPOCH = 0, FO_SEEN = 64, FO_O = FO_SEEN + 256 * 16, FO_P = FO_O + 512 * 32;
constexpr float CS = 0.088388347648318447f * 1.4426950408889634f;
constexpr int PRE_PARTS = 3;
#define OUT_AUX 2
constexpr float THR = 8.0f;

#define STAMP(k)

#define MFMA32(a, b, c) __builtin_amdgcn_mfma_f32_32x32x16_bf16(a, b, c, 0, 0, 0)
#define SBAR() __builtin_amdgcn_sched_barrier(0)
#define MEMBAR() asm volatile("" ::: "memory")
#define PIN(x) asm volatile("" : "+v"(x))
#define DI __device__ __forceinline__
#define WAIT_VM(n) asm volatile("s_waitcnt vmcnt(" #n ")" ::: "memory")
#define WAIT_LGKM0() asm volatile("s_waitcnt lgkmcnt(0)" ::: "memory")
#define RAWBAR() do { WAIT_LGKM0(); __builtin_amdgcn_s_barrier(); MEMBAR(); } while (0)

DI unsigned cvtpk(float lo, float hi) { f32x2 v = {lo, hi}; return __builtin_bit_cast(unsigned, __builtin_convertvector(v, bf16x2n)); }
DI s16x4 trd(lds_cptr p) { return __builtin_amdgcn_ds_read_tr16_b64_v4i16((LAS s16x4*)p); }
DI bf16x8 cat(s16x4 a, s16x4 b) { return __builtin_shufflevector(a, b, 0, 1, 2, 3, 4, 5, 6, 7); }
template <bool SCALE> DI bf16x8 pack8(const f32x16& x, int s) {
  const float c = SCALE ? CS : 1.0f;
  u32x4 w = {cvtpk(x[8 * s] * c, x[8 * s + 1] * c), cvtpk(x[8 * s + 2] * c, x[8 * s + 3] * c), cvtpk(x[8 * s + 4] * c, x[8 * s + 5] * c), cvtpk(x[8 * s + 6] * c, x[8 * s + 7] * c)};
  return __builtin_bit_cast(bf16x8, w);
}
DI unsigned rfl(unsigned v) { return __builtin_amdgcn_readfirstlane(v); }
DI unsigned lds_addr(lds_flag f) { return (unsigned)(uintptr_t)f; }

DI void wait1(unsigned early, lds_flag f, unsigned need) {
  unsigned t, sv, cnt;
  asm volatile("s_cmp_ge_u32 %3, %4\n\ts_cbranch_scc1 W1E%=\n\ts_mov_b32 %2, 0\n"
               "W1L%=:\n\tds_read_b32 %0, %5\n\ts_waitcnt lgkmcnt(0)\n\tv_readfirstlane_b32 %1, %0\n\ts_cmp_ge_u32 %1, %4\n\ts_cbranch_scc1 W1E%=\n\t"
               "s_sleep 1\n\ts_add_u32 %2, %2, 1\n\ts_cmp_lt_u32 %2, 0x40000\n\ts_cbranch_scc1 W1L%=\n"
               "W1E%=:"
               : "=&v"(t), "=&s"(sv), "=&s"(cnt) : "s"(early), "s"(need), "v"(lds_addr(f)) : "memory", "scc");
}
DI void wait4(lds_flag f, unsigned need) {
  unsigned a0, a1, a2, m, sv, cnt;
  asm volatile("s_mov_b32 %5, 0\n"
               "W4L%=:\n\tds_read_b32 %3, %7\n\tds_read_b32 %0, %7 offset:4\n\tds_read_b32 %1, %7 offset:8\n\tds_read_b32 %2, %7 offset:12\n\ts_waitcnt lgkmcnt(0)\n\t"
               "v_min_u32 %3, %3, %0\n\tv_min3_u32 %3, %3, %1, %2\n\ts_nop 0\n\tv_readfirstlane_b32 %4, %3\n\ts_cmp_ge_u32 %4, %6\n\ts_cbranch_scc1 W4E%=\n\t"
               "s_sleep 1\n\ts_add_u32 %5, %5, 1\n\ts_cmp_lt_u32 %5, 0x40000\n\ts_cbranch_scc1 W4L%=\n"
               "W4E%=:"
               : "=&v"(a0), "=&v"(a1), "=&v"(a2), "=&v"(m), "=&s"(sv), "=&s"(cnt) : "s"(need), "v"(lds_addr(f)) : "memory", "scc");
}
DI unsigned min4(lds_flag f) { u32x4 v = *(volatile LAS u32x4*)f; unsigned m = v[0] < v[1] ? v[0] : v[1]; m = m < v[2] ? m : v[2]; return rfl(m < v[3] ? m : v[3]); }
DI unsigned lds_add_ret(lds_flag f, int lane) {
  unsigned old = 0;
  if (lane == 0) old = __hip_atomic_fetch_add((LAS unsigned*)f, 1u, __ATOMIC_RELAXED, __HIP_MEMORY_SCOPE_WORKGROUP);
  return rfl(old);
}
DI unsigned gld(const unsigned* p) { return __hip_atomic_load(p, __ATOMIC_RELAXED, __HIP_MEMORY_SCOPE_AGENT); }
DI void gst(unsigned* p, unsigned v) { __hip_atomic_store(p, v, __ATOMIC_RELAXED, __HIP_MEMORY_SCOPE_AGENT); }
DI void gwait(const unsigned* p, unsigned tag) {
  for (unsigned spins = 0; spins < (1u << 15); ++spins) {
    if (__all(gld(p) == tag)) break;
    __builtin_amdgcn_s_sleep(2);
  }
  MEMBAR();
}

#define DMA_HEAD "s_nop 2\n\ts_mov_b32 m0, %0\n\ts_nop 0\n\t"
#define DMA_BODY(POL) \
  if constexpr (N == 1) asm volatile(DMA_HEAD "buffer_load_dwordx4 %1, %2, %3 offen" POL " lds" ::"s"(dst), "v"(va), "s"(srd), "s"(soff) : "m0", "memory"); \
  else if constexpr (N == 2) asm volatile(DMA_HEAD "buffer_load_dwordx4 %1, %3, %4 offen" POL " lds\n\tbuffer_load_dwordx4 %2, %3, %4 offen offset:1024" POL " lds" \
                                          ::"s"(dst), "v"(va), "v"(vb), "s"(srd), "s"(soff) : "m0", "memory"); \
  else if constexpr (N == 3) asm volatile(DMA_HEAD "buffer_load_dwordx4 %1, %4, %5 offen" POL " lds\n\tbuffer_load_dwordx4 %2, %4, %5 offen offset:1024" POL " lds\n\t" \
                                          "buffer_load_dwordx4 %3, %4, %5 offen offset:2048" POL " lds" ::"s"(dst), "v"(va), "v"(vb), "v"(vc), "s"(srd), "s"(soff) : "m0", "memory"); \
  else asm volatile(DMA_HEAD "buffer_load_dwordx4 %1, %5, %6 offen" POL " lds\n\tbuffer_load_dwordx4 %2, %5, %6 offen offset:1024" POL " lds\n\t" \
                    "buffer_load_dwordx4 %3, %5, %6 offen offset:2048" POL " lds\n\tbuffer_load_dwordx4 %4, %5, %6 offen offset:3072" POL " lds" \
                    ::"s"(dst), "v"(va), "v"(vb), "v"(vc), "v"(vd), "s"(srd), "s"(soff) : "m0", "memory");
template <int N, int POL>
DI void dma(__amdgpu_buffer_rsrc_t srd, unsigned soff, unsigned dst, unsigned va, unsigned vb = 0, unsigned vc = 0, unsigned vd = 0) {
  dst = rfl(dst); soff = rfl(soff);
  if constexpr (POL == 0) { DMA_BODY("") } else if constexpr (POL == 1) { DMA_BODY(" nt") } else { DMA_BODY(" sc1") }
}
template <int PITCH> DI unsigned dma_voff(int i) { return (i >> 4) * PITCH + 16 * ((i & 15) ^ ((i >> 4) << 2)); }

DI int wslot(int t) { return (t % 3) * WSLOT; }
DI void qproj(lds_cptr lds, int i, int lane, f32x16 (&acc)[2][3]) {
  const int hi = lane >> 5, blk = (lane >> 4) & 1, q = (lane & 15) >> 2, pp = lane & 3, r32 = lane & 31;
  lds_cptr aw[4], ax[4];
#pragma unroll
  for (int d = 0; d < 2; ++d)
#pragma unroll
    for (int t = 0; t < 2; ++t)
      aw[2 * d + t] = lds + 16384 * (i >> 1) + 2048 * hi + 1024 * t + 256 * q + 16 * (4 * ((2 * (i & 1) + d) ^ q) + ((2 * blk + (pp & 1)) ^ (2 * hi + t))) + 8 * (pp >> 1);
#pragma unroll
  for (int u = 0; u < 4; ++u) ax[u] = lds + 32768 + 128 * r32 + 16 * ((2 * u + hi) ^ ((r32 >> 1) & 7));
  bf16x8 wf[2][2], xf[2][3];
  auto ldw = [&](int buf, int so, int u, int d) { wf[buf][d] = cat(trd(aw[2 * d] + so + 4096 * u), trd(aw[2 * d + 1] + so + 4096 * u)); };
  auto ldx = [&](int buf, int so, int u, int qb) { xf[buf][qb] = *(const LAS bf16x8*)(ax[u] + so + 4096 * qb); };
  RAWBAR();
  int so = wslot(0);
#pragma unroll
  for (int d = 0; d < 2; ++d) ldw(0, so, 0, d);
#pragma unroll
  for (int qb = 0; qb < 3; ++qb) ldx(0, so, 0, qb);
#pragma unroll 1
  for (int t = 0; t < 10; ++t) {
    const int son = wslot(t + 1);
#pragma unroll
    for (int u = 0; u < 4; ++u) {
      SBAR();
      const int nb = (u + 1) & 1, nso = u < 3 ? so : son, nu = (u + 1) & 3;
#pragma unroll
      for (int g = 0; g < 6; ++g) {
        const int d = g / 3, qb = g % 3;
        acc[d][qb] = MFMA32(wf[u & 1][d], xf[u & 1][qb], acc[d][qb]);
        if (u < 3) {
          if (g == 0) ldw(nb, nso, nu, 0);
          else if (g < 4) ldx(nb, nso, nu, g - 1);
          else if (g == 4) ldw(nb, nso, nu, 1);
        } else {
          if (g == 2) { SBAR(); RAWBAR(); ldw(nb, nso, nu, 0); ldx(nb, nso, nu, 0); }
          else if (g == 3) { ldx(nb, nso, nu, 1); ldx(nb, nso, nu, 2); }
          else if (g == 4) ldw(nb, nso, nu, 1);
        }
        SBAR();
      }
    }
    so = son;
  }
}

struct AAddr { lds_cptr k[8]; lds_cptr v[8]; };
DI void aaddr(AAddr& a, lds_cptr kv, int lane) {
  const int r32 = lane & 31, hi = lane >> 5, blk = (lane >> 4) & 1, q = (lane & 15) >> 2, p = lane & 3;
  const int f = ((r32 & 3) << 2) | ((r32 >> 2) & 3);
#pragma unroll
  for (int s = 0; s < 8; ++s) a.k[s] = kv + 256 * r32 + 16 * ((2 * s + hi) ^ f);
#pragma unroll
  for (int c = 0; c < 4; ++c)
#pragma unroll
    for (int t = 0; t < 2; ++t)
      a.v[2 * c + t] = kv + 2 * SLOT + 2048 * t + 1024 * hi + 256 * q + 16 * (4 * (c ^ q) + ((2 * blk + (p >> 1)) ^ (2 * t + hi))) + 8 * (p & 1);
}
DI float max3(float a, float b, float c) { return __builtin_fmaxf(__builtin_fmaxf(a, b), c); }
DI void attn_head(AAddr& a, lds_flag fl, int n0  , lds_flag prog, int lane, bf16x8 (&qf)[8], lds_cptr qnext,
                  __amdgpu_buffer_rsrc_t so, unsigned ooff, unsigned* pend_flag, unsigned tag, LAS char* olds  , unsigned olds_need, lds_flag olds_cnt) {
  f32x16 o[4] = {};
  f32x16 ol = {};
  f32x16 negr = {};
  const bf16x8 ones = {0x3F80, 0x3F80, 0x3F80, 0x3F80, 0x3F80, 0x3F80, 0x3F80, 0x3F80};
  bf16x8 fr[2][4];
  auto ldk = [&](int step, int i) {
    const int tt = step >> 2, kb = (step >> 1) & 1, hf = step & 1;
    fr[step & 1][i] = *(const LAS bf16x8*)(a.k[4 * hf + i] + SLOT * tt + 8192 * kb);
  };
  auto ldv = [&](int step, int c) {
    const int tt = step >> 2, ks = step & 3;
    fr[step & 1][c] = cat(trd(a.v[2 * c] + SLOT * tt + 4096 * ks), trd(a.v[2 * c + 1] + SLOT * tt + 4096 * ks));
  };
  wait1(0, fl + F_KVL, n0 + 1);
#pragma unroll
  for (int i = 0; i < 4; ++i) ldk(0, i);
#pragma unroll 1
  for (int ch = 0; ch < 4; ++ch) {
    const int n = n0 + 8 * ch;
    f32x16 s[4];
    unsigned early = 0;
    float mx = -__builtin_inff();
#pragma unroll
    for (int step = 0; step < 8; ++step) {
      if (step & 1) { MEMBAR(); prog[0] = n + (step >> 1) + 1; if (step < 7) wait1(rfl(early), fl + F_KVL, n + (step >> 1) + 2); }
      SBAR();
      const int ti = step >> 1, hf = step & 1;
#pragma unroll
      for (int i = 0; i < 4; ++i) {
        if (hf == 0 && i == 0) s[ti] = MFMA32(fr[step & 1][0], qf[0], negr);
        else s[ti] = MFMA32(fr[step & 1][i], qf[4 * hf + i], s[ti]);
        if (step + 1 < 8) ldk(step + 1, i);
        if ((!(step & 1) || step == 7) && i == 0) early = fl[F_KVL];
        if (ti >= 1 && (hf == 1 || i >= 2)) {
          const int g = hf == 0 ? i - 2 : 2 + i;
          const f32x16& x = s[ti - 1];
          if (g < 2) { mx = max3(mx, x[4 * g], x[4 * g + 1]); mx = max3(mx, x[4 * g + 2], x[4 * g + 3]); }
          else mx = max3(mx, x[8 + 2 * (g - 2)], x[9 + 2 * (g - 2)]);
          PIN(mx);
        }
        SBAR();
      }
    }
#pragma unroll
    for (int i = 0; i < 8; ++i) a.k[i] += (ch & 1) ? 65536 : -65536;
    wait1(rfl(early), fl + F_KVL, n + 5);
#pragma unroll
    for (int c = 0; c < 4; ++c) ldv(0, c);
    SBAR();
#pragma unroll
    for (int r = 0; r < 16; r += 2) mx = max3(mx, s[3][r], s[3][r + 1]);
    { auto rr = __builtin_amdgcn_permlane32_swap(__float_as_uint(mx), __float_as_uint(mx), false, false);
      mx = __builtin_fmaxf(__uint_as_float(rr[0]), __uint_as_float(rr[1])); }
    if (ch == 0 || __any(mx > THR)) {
      const float dl = ch == 0 ? mx : __builtin_fmaxf(mx, 0.f);
#pragma unroll
      for (int i = 0; i < 4; ++i)
#pragma unroll
        for (int r = 0; r < 16; ++r) s[i][r] -= dl;
#pragma unroll
      for (int r = 0; r < 16; ++r) negr[r] -= dl;
      if (ch > 0) {
        const float alpha = __builtin_amdgcn_exp2f(-dl);
        ol[0] *= alpha;
#pragma unroll
        for (int c = 0; c < 4; ++c)
#pragma unroll
          for (int r = 0; r < 16; ++r) o[c][r] *= alpha;
      }
    }
    unsigned pw[3][4];
    float pe[2][2];
    auto expo = [&](int step, int i) {
      const f32x16& x = s[step >> 1];
      const int e = 8 * (step & 1) + 2 * i;
      pe[i & 1][0] = __builtin_amdgcn_exp2f(x[e]); pe[i & 1][1] = __builtin_amdgcn_exp2f(x[e + 1]);
      PIN(pe[i & 1][0]); PIN(pe[i & 1][1]);
    };
    auto packw = [&](int step, int i) { pw[step % 3][i] = cvtpk(pe[i & 1][0], pe[i & 1][1]); PIN(pw[step % 3][i]); };
#pragma unroll
    for (int st = 0; st < 2; ++st)
#pragma unroll
      for (int i = 0; i < 4; ++i) { expo(st, i); packw(st, i); }
    SBAR();
#pragma unroll
    for (int step = 0; step < 8; ++step) {
      if (step & 1) { MEMBAR(); prog[0] = n + 4 + (step >> 1) + 1; wait1(rfl(early), fl + F_KVL, (step < 7 || ch < 3) ? n + 4 + (step >> 1) + 2 : 0); }
      SBAR();
      u32x4 pv = {pw[step % 3][0], pw[step % 3][1], pw[step % 3][2], pw[step % 3][3]};
      const bf16x8 pf = __builtin_bit_cast(bf16x8, pv);
#pragma unroll
      for (int c = 0; c < 4; ++c) {
        o[c] = MFMA32(fr[step & 1][c], pf, o[c]);
        if (step + 1 < 8) ldv(step + 1, c);
        else ldk(0, c);
        if (step + 2 < 8) { if (c > 0) packw(step + 2, c - 1); expo(step + 2, c); }
        if (!(step & 1) && c == 0) early = fl[F_KVL];
        SBAR();
      }
      ol = MFMA32(ones, pf, ol);
      if (step + 2 < 8) packw(step + 2, 3);
      SBAR();
    }
#pragma unroll
    for (int i = 0; i < 8; ++i) a.v[i] += (ch & 1) ? 65536 : -65536;
    if (ch == 0 && pend_flag) {
      WAIT_VM(0);
      if (lds_add_ret(fl + F_OC, lane) == 2 && lane == 0) gst(pend_flag, tag);
    }
  }
  const float inv = 1.0f / ol[0];
  wait4(fl + F_KVP, olds_need);
  if (qnext) {
#pragma unroll
    for (int s = 0; s < 8; ++s) qf[s] = *(const LAS bf16x8*)(qnext + s * 1024 + lane * 16);
#pragma unroll
    for (int s = 0; s < 8; ++s) PIN(qf[s]);
    WAIT_LGKM0();
  }
#pragma unroll
  for (int c = 0; c < 4; ++c) {
#pragma unroll
    for (int r = 0; r < 16; ++r) o[c][r] *= inv;
    const bf16x8 f0 = pack8<false>(o[c], 0), f1 = pack8<false>(o[c], 1);
    *(LAS bf16x8*)(olds + (2 * c) * 1024 + lane * 16) = f0;
    *(LAS bf16x8*)(olds + (2 * c + 1) * 1024 + lane * 16) = f1;
    __builtin_amdgcn_raw_buffer_store_b128(__builtin_bit_cast(u32x4, f0), so, ooff + lane * 16 + (2 * c) * 1024, 0, 16);
    __builtin_amdgcn_raw_buffer_store_b128(__builtin_bit_cast(u32x4, f1), so, ooff + lane * 16 + (2 * c + 1) * 1024, 0, 16);
  }
  WAIT_LGKM0();
  lds_add_ret(olds_cnt, lane);
}

__global__ __launch_bounds__(512, 2) void mha_fused(const char* __restrict__ x, const char* __restrict__ kc, const char* __restrict__ vc, const char* __restrict__ wq,
                                                     const char* __restrict__ wo, unsigned short* __restrict__ out, char* __restrict__ ws) {
  extern __shared__ __attribute__((aligned(16))) char lds[];
  const int tid = threadIdx.x, lane = tid & 63, wave = rfl(tid >> 6);
  const int bid = blockIdx.x, xcd = bid & 7, idx = bid >> 3;
  const int hg = xcd >> 1, hl = idx & 3;
  const int b = 8 * (xcd & 1) + (idx >> 2), h0 = 4 * hg + hl;
  unsigned long long* dbg = (unsigned long long*)(ws + (128u << 20)); (void)dbg;
  unsigned* gf = (unsigned*)(ws + WS_F);
  if (wave == 0) STAMP(0);
  lds_flag fl = (lds_flag)(lds + LDS_FLAG);
  const unsigned lds0 = (unsigned)(uintptr_t)(LAS char*)lds;

  const __amdgpu_buffer_rsrc_t sows = __builtin_amdgcn_make_buffer_rsrc((void*)ws, 0, WS_P + WS_P_BYTES, 0x00020000);
  const __amdgpu_buffer_rsrc_t swo = __builtin_amdgcn_make_buffer_rsrc((void*)wo, 0, HD * D * 2, 0x00020000);
  auto head_of = [&](int r, int i) { return 16 * r + 4 * hg + ((i + hl) & 3); };
  unsigned wv[5];
#pragma unroll
  for (int j = 0; j < 5; ++j) { const int pi = 64 * j + lane; wv[j] = (pi / 20) * 1280 + (pi % 20) * 16 - (j < 4 ? 1024 * j : 0); }
  auto issue_w = [&](int h, int buf, int part) {
    const unsigned sb = (unsigned)(h * 128 + 16 * part) * 1280u + hl * 320u, dst = lds0 + buf + part * 5120;
    dma<4, 0>(swo, sb, dst, wv[0], wv[1], wv[2], wv[3]); dma<1, 0>(swo, sb, dst + 4096, wv[4]);
  };

  if (wave == 7) {
    __builtin_amdgcn_s_setprio(3);
    const __amdgpu_buffer_rsrc_t sk = __builtin_amdgcn_make_buffer_rsrc((void*)kc, 0, B * H * SK * DH * 2, 0x00020000);
    const __amdgpu_buffer_rsrc_t sv = __builtin_amdgcn_make_buffer_rsrc((void*)vc, 0, B * H * SK * DH * 2, 0x00020000);
    const unsigned v0 = dma_voff<256>(lane);
    auto kvunit = [&](int u) {
      const int c = u >> 3, head = h0 + 16 * (c >> 2), isv = (u >> 2) & 1;
      const unsigned sbase = (unsigned)((b * H + head) * SK + (c & 3) * 128 + (u & 3) * 32) * 256u;
      const unsigned dst = lds0 + ((c & 1) ? 0 : 65536) + (u & 7) * 8192;
      dma<4, 1>(isv ? sv : sk, sbase, dst, v0, v0 ^ 16, v0 ^ 32, v0 ^ 48);
      dma<4, 1>(isv ? sv : sk, sbase + 4096, dst + 4096, v0, v0 ^ 16, v0 ^ 32, v0 ^ 48);
    };
#pragma unroll 1
    for (int t = 0; t < 10; ++t) RAWBAR();
    unsigned freed = 0;
    int P = 0;
#pragma unroll 1
    for (int u = 0; u < 64; ++u) {
      const unsigned need = u < 8 ? 0 : u < 16 ? 1 : u - 15;
      if (freed < need) {
        freed = min4(fl + F_KVP);
        if (freed < need) {
          if (P < u) { WAIT_VM(0); P = u; fl[F_KVL] = P; }
          wait4(fl + F_KVP, need);
          freed = need;
        }
      }
      kvunit(u);
      if (u == 1 || u == 3) RAWBAR();
      WAIT_VM(24);
      if (P < u - 2) { P = u - 2; fl[F_KVL] = P; }
    }
    WAIT_VM(8); fl[F_KVL] = 63;
    WAIT_VM(0); fl[F_KVL] = 64;
    __builtin_amdgcn_s_setprio(0);
    STAMP(5);
  } else if (wave <= 3) {
    f32x16 acc[2][3] = {};
    qproj((lds_cptr)lds, wave, lane, acc);
#pragma unroll
    for (int d = 0; d < 2; ++d)
#pragma unroll
      for (int qb = 0; qb < 3; ++qb) {
        LAS char* qx = (LAS char*)lds + ((wave >> 1) ? LDS_QB : LDS_Q0) + qb * 8192 + (2 * (2 * (wave & 1) + d)) * 1024 + lane * 16;
        *(LAS bf16x8*)qx = pack8<true>(acc[d][qb], 0);
        *(LAS bf16x8*)(qx + 1024) = pack8<true>(acc[d][qb], 1);
      }
    RAWBAR();
  } else if (wave == 4) {
    fl[lane] = (lane == F_KVP + 3) ? 0x7fffffffu : 0u;
    const unsigned tag0 = gld(gf + FO_EPOCH) + 1u;
    fl[F_EP] = tag0;
    if (lane == 0) gst(gf + FO_SEEN + bid * 16, tag0);
#pragma unroll 1
    for (int t = 0; t < 9; ++t) RAWBAR();
    unsigned wrm = 0;
#pragma unroll
    for (int i = 0; i < 5; ++i) {
      const int L = idx * 320 + i * 64 + lane, hh = L / 1280;
      wrm ^= *(const unsigned*)(wo + (size_t)(16 * (hh >> 2) + 4 * hg + (hh & 3)) * 163840 + (size_t)(L - hh * 1280) * 128);
    }
#pragma unroll 1
    for (int t = 9; t < 12; ++t) RAWBAR();
    asm volatile("" :: "v"(wrm));
    if (bid == 0) {
#pragma unroll
      for (int i = 0; i < 4; ++i) gwait(gf + FO_SEEN + (i * 64 + lane) * 16, tag0);
      if (lane == 0) gst(gf + FO_EPOCH, tag0);
    }
  } else {
    __builtin_amdgcn_s_setprio(3);
    const int pt = wave - 5;
    const int c0 = ((b & 7) + 2 * hl) % 10;
    const __amdgpu_buffer_rsrc_t sw = __builtin_amdgcn_make_buffer_rsrc((void*)wq, 0, D * HD * 2, 0x00020000);
    const __amdgpu_buffer_rsrc_t sx = __builtin_amdgcn_make_buffer_rsrc((void*)x, 0, B * SQ * D * 2, 0x00020000);
    const unsigned v0 = dma_voff<8192>(lane);
    const unsigned xv0 = (lane >> 3) * 1280 + (((lane & 7) ^ (lane >> 4)) * 16);
    auto issue = [&](int t) {
      const int cc = t + c0, c = cc >= 10 ? cc - 10 : cc;
      const unsigned sbase = (unsigned)(c * 64) * 8192u + (h0 + 16 * pt) * 256u;
      const unsigned dst = lds0 + wslot(t);
      const unsigned xs = (unsigned)(b * SQ + 48 * pt) * 1280u + c * 128u;
      dma<4, 0>(sx, xs, dst + 32768 + 6 * pt * 1024, xv0, (xv0 ^ 64) + 9216, xv0 + 2 * 9216, (xv0 ^ 64) + 3 * 9216);
      dma<2, 0>(sx, xs + 4 * 10240, dst + 32768 + (6 * pt + 4) * 1024, xv0, (xv0 ^ 64) + 9216);
#pragma unroll
      for (int j = 0; j < 4; ++j)
        dma<4, 0>(sw, sbase + j * 4 * 32768, dst + pt * 16384 + j * 4096, v0, (v0 ^ 16) + 31744, (v0 ^ 32) + 2 * 31744, (v0 ^ 48) + 3 * 31744);
    };
    issue(0); issue(1);
    WAIT_VM(22); issue(2); RAWBAR();
#pragma unroll 1
    for (int t = 0; t < 10; ++t) {
      if (t < 8) WAIT_VM(22); else WAIT_VM(0);
      RAWBAR();
      if (t + 3 < 10) issue(t + 3);
    }
    RAWBAR();
    __builtin_amdgcn_s_setprio(0);
  }
  const unsigned tag = rfl(fl[F_EP]);

  if (wave <= 2) {
    __builtin_amdgcn_s_setprio(1);
    const int qb = wave;
    bf16x8 qf[8];
    if (wave == 0) STAMP(1);
    AAddr aa; aaddr(aa, (lds_cptr)lds + 65536, lane);
    {
      lds_cptr qx = (lds_cptr)lds + LDS_Q0 + qb * 8192;
#pragma unroll
      for (int s = 0; s < 8; ++s) qf[s] = *(const LAS bf16x8*)(qx + s * 1024 + lane * 16);
#pragma unroll
      for (int s = 0; s < 8; ++s) PIN(qf[s]);
      WAIT_LGKM0();
    }
#pragma unroll 1
    for (int r = 0; r < 2; ++r) {
      attn_head(aa, fl, 32 * r, fl + F_KVP + qb, lane, qf, r == 0 ? (lds_cptr)lds + LDS_QB + qb * 8192 : (lds_cptr) nullptr,
                sows, (unsigned)(((b * H + h0 + 16 * r) * 3 + qb) * 8192), r == 1 ? gf + FO_O + (b * H + h0) * 32 : nullptr, tag,
                (LAS char*)lds + (r ? T_O1 : LDS_QB) + qb * 8192, r ? 56u : 0u, fl + F_OL + r);
      if (wave == 0) STAMP(2 + 2 * r);
    }
    __builtin_amdgcn_s_setprio(0);
  } else {
    if (wave == 3) {
      wait1(0, fl + F_OL, 3);
      gwait(gf + FO_O + (b * H + head_of(0, lane & 3)) * 32, tag);
    } else if (wave < 7) {
      wait4(fl + F_KVP, 56);
#pragma unroll
      for (int p = 0; p < PRE_PARTS; ++p) if (3 * p + (wave - 4) < 8) issue_w(head_of(0, 0), T_W0, 3 * p + (wave - 4));
      WAIT_VM(0);
    }
  }

  {
    const int g = wave & 3, kh = wave >> 2;
    const int hi = lane >> 5, blk = (lane >> 4) & 1, q = (lane & 15) >> 2, p = lane & 3;
    const int aoff = 320 * (64 * kh + 4 * hi + q) + 32 * blk + 8 * p + 64 * g;
    const int gx = g < 3 ? g : 0;
    f32x16 acc[4] = {};
    auto issue_o = [&](int h, int slot) {
      dma<3, 2>(sows, (unsigned)((b * H + h) * 24576 + wave * 3072), lds0 + slot + wave * 3072, lane * 16, lane * 16, lane * 16);
    };
    auto head = [&](int wbuf, int oslot) {
      lds_cptr a0 = (lds_cptr)lds + wbuf + aoff, a1 = a0 + 64 * (4 - g);
      lds_cptr ob = (lds_cptr)lds + oslot + kh * 4096 + lane * 16;
      bf16x8 af[2][2], bf[2][4];
      auto lda = [&](int buf, int k, int i) { lds_cptr o = (i ? a1 : a0) + 320 * 16 * k; af[buf][i] = cat(trd(o), trd(o + 320 * 8)); };
      auto ldb = [&](int buf, int k, int i) { bf[buf][i] = *(const LAS bf16x8*)(ob + (i < 3 ? i : gx) * 8192 + k * 1024); };
      lda(0, 0, 0); ldb(0, 0, 0); ldb(0, 0, 1); ldb(0, 0, 2); lda(0, 0, 1); ldb(0, 0, 3);
#pragma unroll
      for (int k = 0; k < 4; ++k) {
        SBAR();
#pragma unroll
        for (int i = 0; i < 4; ++i) {
          acc[i] = MFMA32(af[k & 1][i == 3], bf[k & 1][i], acc[i]);
          if (k < 3) { ldb((k + 1) & 1, k + 1, i); if (i < 2) lda((k + 1) & 1, k + 1, i); }
          SBAR();
        }
      }
    };
    const int sr[8] = {0, 0, 1, 0, 0, 1, 1, 1}, si[8] = {0, 1, 0, 2, 3, 1, 2, 3};
    const int sl[8] = {LDS_QB, T_OA, T_O1, LDS_QB, T_OA, T_O1, LDS_QB, T_OA};
    RAWBAR();
    if (wave == 0) STAMP(6);
    issue_o(head_of(0, 1), T_OA);
    unsigned pollv = 0;
#pragma unroll
    for (int s = 0; s < 8; ++s) {
      if (s + 1 < 8) issue_w(head_of(sr[s + 1], si[s + 1]), ((s + 1) & 1) ? T_W1 : T_W0, wave);
      if (s >= 1 && s + 2 < 8) issue_o(head_of(sr[s + 2], si[s + 2]), sl[s + 2]);
      if (s == 0 && wave <= 2) {
        WAIT_VM(8);
        if (lds_add_ret(fl + F_OC + 1, lane) == 2 && lane == 0) gst(gf + FO_O + (b * H + h0 + 16) * 32, tag);
      }
      if (s == 1 && wave == 3) pollv = gld(gf + FO_O + (b * H + head_of(1, lane & 3)) * 32);
      head((s & 1) ? T_W1 : T_W0, sl[s]);
      if (s == 2 && wave == 3) {
        if (!__all(pollv == tag)) gwait(gf + FO_O + (b * H + head_of(1, lane & 3)) * 32, tag);
      }
      if (s + 1 < 8) {
        if (s >= 1 && s + 2 < 8) WAIT_VM(3); else WAIT_VM(0);
        RAWBAR();
      }
    }
    if (wave == 0) STAMP(7);
    RAWBAR();
    {
      LAS char* xch = (LAS char*)lds + wave * 8192 + lane * 16;
#pragma unroll
      for (int i = 0; i < 2; ++i)
#pragma unroll
        for (int j = 0; j < 4; ++j) { const f32x16& t = acc[kh ? i : 2 + i]; f32x4 v = {t[4 * j], t[4 * j + 1], t[4 * j + 2], t[4 * j + 3]}; *(LAS f32x4*)(xch + (i * 4 + j) * 1024) = v; }
      RAWBAR();
      lds_cptr xr = (lds_cptr)lds + (wave ^ 4) * 8192 + lane * 16;
#pragma unroll
      for (int i = 0; i < 2; ++i)
#pragma unroll
        for (int j = 0; j < 4; ++j) { const f32x4 v = *(const LAS f32x4*)(xr + (i * 4 + j) * 1024); f32x16& t = acc[kh ? 2 + i : i];
          t[4 * j] += v[0]; t[4 * j + 1] += v[1]; t[4 * j + 2] += v[2]; t[4 * j + 3] += v[3]; }
    }
    const __amdgpu_buffer_rsrc_t sout = __builtin_amdgcn_make_buffer_rsrc((void*)out, 0, B * SQ * D * 2, 0x00020000);
    const unsigned pbase = WS_P + (unsigned)((b * 4 + hl) * 4) * (15 * 4096);
#pragma unroll
    for (int ii = 0; ii < 2; ++ii) {
      const int i = 2 * kh + ii, tile = i < 3 ? g * 3 + i : 12 + g;
      if ((i < 3 || g < 3) && (tile & 3) != hg) {
        const f32x16& t = kh ? acc[2 + ii] : acc[ii];
#pragma unroll
        for (int j = 0; j < 4; ++j) {
          f32x4 v = {t[4 * j], t[4 * j + 1], t[4 * j + 2], t[4 * j + 3]};
          __builtin_amdgcn_raw_buffer_store_b128(__builtin_bit_cast(u32x4, v), sows, pbase + (hg * 15 + tile) * 4096 + lane * 16 + j * 1024, 0, 16);
        }
      }
    }
    WAIT_VM(0);
    if (lane == 0) gst(gf + FO_P + (((b * 4 + hl) * 4 + hg) * 8 + wave) * 32, tag);
    gwait(gf + FO_P + (((b * 4 + hl) * 4 + ((hg + 1 + (lane % 3)) & 3)) * 8 + wave) * 32, tag);
#pragma unroll
    for (int ii = 0; ii < 2; ++ii) {
      const int i = 2 * kh + ii, tile = i < 3 ? g * 3 + i : 12 + g;
      if ((i < 3 || g < 3) && (tile & 3) == hg) {
        const f32x16& t = kh ? acc[2 + ii] : acc[ii];
        u32x4 pv[3][4];
#pragma unroll
        for (int o = 0; o < 3; ++o)
#pragma unroll
          for (int j = 0; j < 4; ++j) pv[o][j] = __builtin_amdgcn_raw_buffer_load_b128(sows, lane * 16 + j * 1024, pbase + (((hg + 1 + o) & 3) * 15 + tile) * 4096, 16);
        const int nt = i < 3 ? g : 4, qb = i < 3 ? i : g;
        u32x2 w2[4];
#pragma unroll
        for (int j = 0; j < 4; ++j) {
          f32x4 v = {t[4 * j], t[4 * j + 1], t[4 * j + 2], t[4 * j + 3]};
#pragma unroll
          for (int o = 0; o < 3; ++o) v += __builtin_bit_cast(f32x4, pv[o][j]);
          w2[j] = u32x2{cvtpk(v[0], v[1]), cvtpk(v[2], v[3])};
        }
        const unsigned obase = (unsigned)(((b * SQ + 32 * qb + (lane & 31)) * D + 160 * hl + 32 * nt + 8 * hi) * 2);
#pragma unroll
        for (int j = 0; j < 4; j += 2) {
          auto r0 = __builtin_amdgcn_permlane32_swap(w2[j][0], w2[j + 1][0], false, false);
          auto r1 = __builtin_amdgcn_permlane32_swap(w2[j][1], w2[j + 1][1], false, false);
          u32x4 w4 = {r0[0], r1[0], r0[1], r1[1]};
          __builtin_amdgcn_raw_buffer_store_b128(w4, sout, obase + 16 * j, 0, OUT_AUX);
        }
      }
    }
    if (wave == 0) STAMP(8);
  }
}

extern "C" void kernel_launch(void* const* d_in, const int* in_sizes, int n_in, void* d_out, int out_size, void* d_ws, size_t ws_size, hipStream_t stream) {
  (void)hipFuncSetAttribute((const void*)mha_fused, hipFuncAttributeMaxDynamicSharedMemorySize, LDS_BYTES);
  mha_fused<<<256, 512, LDS_BYTES, stream>>>((const char*)d_in[0], (const char*)d_in[1], (const char*)d_in[2], (const char*)d_in[3], (const char*)d_in[4],
                                              (unsigned short*)d_out, (char*)d_ws);
}
```

```cpp
#include <hip/hip_runtime.h>

typedef short bf16x8 __attribute__((ext_vector_type(8)));
typedef short s16x4 __attribute__((ext_vector_type(4)));
typedef float f32x16 __attribute__((ext_vector_type(16)));
typedef float f32x4 __attribute__((ext_vector_type(4)));
typedef float f32x2 __attribute__((ext_vector_type(2)));
typedef unsigned u32x4 __attribute__((ext_vector_type(4)));
typedef unsigned u32x2 __attribute__((ext_vector_type(2)));
typedef __bf16 bf16x2n __attribute__((ext_vector_type(2)));
#define LAS __attribute__((address_space(3)))
typedef LAS const char* lds_cptr;
typedef volatile LAS unsigned* lds_flag;

constexpr int B = 16, SQ = 96, D = 640, H = 32, SK = 512, DH = 128, HD = H * DH;
constexpr int SLOT = 16384;
constexpr int WSLOT = 45056;
constexpr int LDS_Q0 = 0, LDS_QB = 131072  , LDS_FLAG = 155648, LDS_BYTES = LDS_FLAG + 256;
constexpr int T_W0 = 65536, T_W1 = 0, T_O1 = 106496, T_OA = 40960;
constexpr int F_KVL = 0, F_KVP = 4, F_EP = 8, F_OC = 12, F_OL = 16;
constexpr unsigned WS_P = 16u << 20, WS_P_BYTES = 16 * 4 * 4 * 15 * 4096, WS_F = 40u << 20;
constexpr unsigned FO_EPOCH = 0, FO_SEEN = 64, FO_O = FO_SEEN + 256 * 16, FO_P = FO_O + 512 * 32;
constexpr float CS = 0.088388347648318447f * 1.4426950408889634f;
constexpr int PRE_PARTS = 3;
#define GSL 1
#define OUT_AUX 2
constexpr float THR = 8.0f;

#define STAMP(k)

#define MFMA32(a, b, c) __builtin_amdgcn_mfma_f32_32x32x16_bf16(a, b, c, 0, 0, 0)
#define SBAR() __builtin_amdgcn_sched_barrier(0)
#define MEMBAR() asm volatile("" ::: "memory")
#define PIN(x) asm volatile("" : "+v"(x))
#define DI __device__ __forceinline__
#define WAIT_VM(n) asm volatile("s_waitcnt vmcnt(" #n ")" ::: "memory")
#define WAIT_LGKM0() asm volatile("s_waitcnt lgkmcnt(0)" ::: "memory")
#define RAWBAR() do { WAIT_LGKM0(); __builtin_amdgcn_s_barrier(); MEMBAR(); } while (0)

DI unsigned cvtpk(float lo, float hi) { f32x2 v = {lo, hi}; return __builtin_bit_cast(unsigned, __builtin_convertvector(v, bf16x2n)); }
DI s16x4 trd(lds_cptr p) { return __builtin_amdgcn_ds_read_tr16_b64_v4i16((LAS s16x4*)p); }
DI bf16x8 cat(s16x4 a, s16x4 b) { return __builtin_shufflevector(a, b, 0, 1, 2, 3, 4, 5, 6, 7); }
template <bool SCALE> DI bf16x8 pack8(const f32x16& x, int s) {
  const float c = SCALE ? CS : 1.0f;
  u32x4 w = {cvtpk(x[8 * s] * c, x[8 * s + 1] * c), cvtpk(x[8 * s + 2] * c, x[8 * s + 3] * c), cvtpk(x[8 * s + 4] * c, x[8 * s + 5] * c), cvtpk(x[8 * s + 6] * c, x[8 * s + 7] * c)};
  return __builtin_bit_cast(bf16x8, w);
}
DI unsigned rfl(unsigned v) { return __builtin_amdgcn_readfirstlane(v); }
DI unsigned lds_addr(lds_flag f) { return (unsigned)(uintptr_t)f; }

DI void wait1(unsigned early, lds_flag f, unsigned need) {
  unsigned t, sv, cnt;
  asm volatile("s_cmp_ge_u32 %3, %4\n\ts_cbranch_scc1 W1E%=\n\ts_mov_b32 %2, 0\n"
               "W1L%=:\n\tds_read_b32 %0, %5\n\ts_waitcnt lgkmcnt(0)\n\tv_readfirstlane_b32 %1, %0\n\ts_cmp_ge_u32 %1, %4\n\ts_cbranch_scc1 W1E%=\n\t"
               "s_sleep 1\n\ts_add_u32 %2, %2, 1\n\ts_cmp_lt_u32 %2, 0x40000\n\ts_cbranch_scc1 W1L%=\n"
               "W1E%=:"
               : "=&v"(t), "=&s"(sv), "=&s"(cnt) : "s"(early), "s"(need), "v"(lds_addr(f)) : "memory", "scc");
}
DI void wait4(lds_flag f, unsigned need) {
  unsigned a0, a1, a2, m, sv, cnt;
  asm volatile("s_mov_b32 %5, 0\n"
               "W4L%=:\n\tds_read_b32 %3, %7\n\tds_read_b32 %0, %7 offset:4\n\tds_read_b32 %1, %7 offset:8\n\tds_read_b32 %2, %7 offset:12\n\ts_waitcnt lgkmcnt(0)\n\t"
               "v_min_u32 %3, %3, %0\n\tv_min3_u32 %3, %3, %1, %2\n\ts_nop 0\n\tv_readfirstlane_b32 %4, %3\n\ts_cmp_ge_u32 %4, %6\n\ts_cbranch_scc1 W4E%=\n\t"
               "s_sleep 1\n\ts_add_u32 %5, %5, 1\n\ts_cmp_lt_u32 %5, 0x40000\n\ts_cbranch_scc1 W4L%=\n"
               "W4E%=:"
               : "=&v"(a0), "=&v"(a1), "=&v"(a2), "=&v"(m), "=&s"(sv), "=&s"(cnt) : "s"(need), "v"(lds_addr(f)) : "memory", "scc");
}
DI unsigned min4(lds_flag f) { u32x4 v = *(volatile LAS u32x4*)f; unsigned m = v[0] < v[1] ? v[0] : v[1]; m = m < v[2] ? m : v[2]; return rfl(m < v[3] ? m : v[3]); }
DI unsigned lds_add_ret(lds_flag f, int lane) {
  unsigned old = 0;
  if (lane == 0) old = __hip_atomic_fetch_add((LAS unsigned*)f, 1u, __ATOMIC_RELAXED, __HIP_MEMORY_SCOPE_WORKGROUP);
  return rfl(old);
}
DI unsigned gld(const unsigned* p) { return __hip_atomic_load(p, __ATOMIC_RELAXED, __HIP_MEMORY_SCOPE_AGENT); }
DI void gst(unsigned* p, unsigned v) { __hip_atomic_store(p, v, __ATOMIC_RELAXED, __HIP_MEMORY_SCOPE_AGENT); }
DI void gwait(const unsigned* p, unsigned tag) {
  for (unsigned spins = 0; spins < (1u << 15); ++spins) {
    if (__all(gld(p) == tag)) break;
    __builtin_amdgcn_s_sleep(GSL);
  }
  MEMBAR();
}

#define DMA_HEAD "s_nop 2\n\ts_mov_b32 m0, %0\n\ts_nop 0\n\t"
#define DMA_BODY(POL) \
  if constexpr (N == 1) asm volatile(DMA_HEAD "buffer_load_dwordx4 %1, %2, %3 offen" POL " lds" ::"s"(dst), "v"(va), "s"(srd), "s"(soff) : "m0", "memory"); \
  else if constexpr (N == 2) asm volatile(DMA_HEAD "buffer_load_dwordx4 %1, %3, %4 offen" POL " lds\n\tbuffer_load_dwordx4 %2, %3, %4 offen offset:1024" POL " lds" \
                                          ::"s"(dst), "v"(va), "v"(vb), "s"(srd), "s"(soff) : "m0", "memory"); \
  else if constexpr (N == 3) asm volatile(DMA_HEAD "buffer_load_dwordx4 %1, %4, %5 offen" POL " lds\n\tbuffer_load_dwordx4 %2, %4, %5 offen offset:1024" POL " lds\n\t" \
                                          "buffer_load_dwordx4 %3, %4, %5 offen offset:2048" POL " lds" ::"s"(dst), "v"(va), "v"(vb), "v"(vc), "s"(srd), "s"(soff) : "m0", "memory"); \
  else asm volatile(DMA_HEAD "buffer_load_dwordx4 %1, %5, %6 offen" POL " lds\n\tbuffer_load_dwordx4 %2, %5, %6 offen offset:1024" POL " lds\n\t" \
                    "buffer_load_dwordx4 %3, %5, %6 offen offset:2048" POL " lds\n\tbuffer_load_dwordx4 %4, %5, %6 offen offset:3072" POL " lds" \
                    ::"s"(dst), "v"(va), "v"(vb), "v"(vc), "v"(vd), "s"(srd), "s"(soff) : "m0", "memory");
template <int N, int POL>
DI void dma(__amdgpu_buffer_rsrc_t srd, unsigned soff, unsigned dst, unsigned va, unsigned vb = 0, unsigned vc = 0, unsigned vd = 0) {
  dst = rfl(dst); soff = rfl(soff);
  if constexpr (POL == 0) { DMA_BODY("") } else if constexpr (POL == 1) { DMA_BODY(" nt") } else { DMA_BODY(" sc1") }
}
template <int PITCH> DI unsigned dma_voff(int i) { return (i >> 4) * PITCH + 16 * ((i & 15) ^ ((i >> 4) << 2)); }

DI int wslot(int t) { return (t % 3) * WSLOT; }
DI void qproj(lds_cptr lds, int i, int lane, f32x16 (&acc)[2][3]) {
  const int hi = lane >> 5, blk = (lane >> 4) & 1, q = (lane & 15) >> 2, pp = lane & 3, r32 = lane & 31;
  lds_cptr aw[4], ax[4];
#pragma unroll
  for (int d = 0; d < 2; ++d)
#pragma unroll
    for (int t = 0; t < 2; ++t)
      aw[2 * d + t] = lds + 16384 * (i >> 1) + 2048 * hi + 1024 * t + 256 * q + 16 * (4 * ((2 * (i & 1) + d) ^ q) + ((2 * blk + (pp & 1)) ^ (2 * hi + t))) + 8 * (pp >> 1);
#pragma unroll
  for (int u = 0; u < 4; ++u) ax[u] = lds + 32768 + 128 * r32 + 16 * ((2 * u + hi) ^ ((r32 >> 1) & 7));
  bf16x8 wf[2][2], xf[2][3];
  auto ldw = [&](int buf, int so, int u, int d) { wf[buf][d] = cat(trd(aw[2 * d] + so + 4096 * u), trd(aw[2 * d + 1] + so + 4096 * u)); };
  auto ldx = [&](int buf, int so, int u, int qb) { xf[buf][qb] = *(const LAS bf16x8*)(ax[u] + so + 4096 * qb); };
  RAWBAR();
  int so = wslot(0);
#pragma unroll
  for (int d = 0; d < 2; ++d) ldw(0, so, 0, d);
#pragma unroll
  for (int qb = 0; qb < 3; ++qb) ldx(0, so, 0, qb);
#pragma unroll 1
  for (int t = 0; t < 10; ++t) {
    const int son = wslot(t + 1);
#pragma unroll
    for (int u = 0; u < 4; ++u) {
      SBAR();
      const int nb = (u + 1) & 1, nso = u < 3 ? so : son, nu = (u + 1) & 3;
#pragma unroll
      for (int g = 0; g < 6; ++g) {
        const int d = g / 3, qb = g % 3;
        acc[d][qb] = MFMA32(wf[u & 1][d], xf[u & 1][qb], acc[d][qb]);
        if (u < 3) {
          if (g == 0) ldw(nb, nso, nu, 0);
          else if (g < 4) ldx(nb, nso, nu, g - 1);
          else if (g == 4) ldw(nb, nso, nu, 1);
        } else {
          if (g == 2) { SBAR(); RAWBAR(); ldw(nb, nso, nu, 0); ldx(nb, nso, nu, 0); }
          else if (g == 3) { ldx(nb, nso, nu, 1); ldx(nb, nso, nu, 2); }
          else if (g == 4) ldw(nb, nso, nu, 1);
        }
        SBAR();
      }
    }
    so = son;
  }
}

struct AAddr { lds_cptr k[8]; lds_cptr v[8]; };
DI void aaddr(AAddr& a, lds_cptr kv, int lane) {
  const int r32 = lane & 31, hi = lane >> 5, blk = (lane >> 4) & 1, q = (lane & 15) >> 2, p = lane & 3;
  const int f = ((r32 & 3) << 2) | ((r32 >> 2) & 3);
#pragma unroll
  for (int s = 0; s < 8; ++s) a.k[s] = kv + 256 * r32 + 16 * ((2 * s + hi) ^ f);
#pragma unroll
  for (int c = 0; c < 4; ++c)
#pragma unroll
    for (int t = 0; t < 2; ++t)
      a.v[2 * c + t] = kv + 2 * SLOT + 2048 * t + 1024 * hi + 256 * q + 16 * (4 * (c ^ q) + ((2 * blk + (p >> 1)) ^ (2 * t + hi))) + 8 * (p & 1);
}
DI float max3(float a, float b, float c) { return __builtin_fmaxf(__builtin_fmaxf(a, b), c); }
DI void attn_head(AAddr& a, lds_flag fl, int n0  , lds_flag prog, int lane, bf16x8 (&qf)[8], lds_cptr qnext,
                  __amdgpu_buffer_rsrc_t so, unsigned ooff, unsigned* pend_flag, unsigned tag, LAS char* olds  , unsigned olds_need, lds_flag olds_cnt) {
  f32x16 o[4] = {};
  f32x16 ol = {};
  f32x16 negr = {};
  const bf16x8 ones = {0x3F80, 0x3F80, 0x3F80, 0x3F80, 0x3F80, 0x3F80, 0x3F80, 0x3F80};
  bf16x8 fr[2][4];
  auto ldk = [&](int step, int i) {
    const int tt = step >> 2, kb = (step >> 1) & 1, hf = step & 1;
    fr[step & 1][i] = *(const LAS bf16x8*)(a.k[4 * hf + i] + SLOT * tt + 8192 * kb);
  };
  auto ldv = [&](int step, int c) {
    const int tt = step >> 2, ks = step & 3;
    fr[step & 1][c] = cat(trd(a.v[2 * c] + SLOT * tt + 4096 * ks), trd(a.v[2 * c + 1] + SLOT * tt + 4096 * ks));
  };
  wait1(0, fl + F_KVL, n0 + 1);
#pragma unroll
  for (int i = 0; i < 4; ++i) ldk(0, i);
#pragma unroll 1
  for (int ch = 0; ch < 4; ++ch) {
    const int n = n0 + 8 * ch;
    f32x16 s[4];
    unsigned early = 0;
    float mx = -__builtin_inff();
#pragma unroll
    for (int step = 0; step < 8; ++step) {
      if (step & 1) { MEMBAR(); prog[0] = n + (step >> 1) + 1; if (step < 7) wait1(rfl(early), fl + F_KVL, n + (step >> 1) + 2); }
      SBAR();
      const int ti = step >> 1, hf = step & 1;
#pragma unroll
      for (int i = 0; i < 4; ++i) {
        if (hf == 0 && i == 0) s[ti] = MFMA32(fr[step & 1][0], qf[0], negr);
        else s[ti] = MFMA32(fr[step & 1][i], qf[4 * hf + i], s[ti]);
        if (step + 1 < 8) ldk(step + 1, i);
        if ((!(step & 1) || step == 7) && i == 0) early = fl[F_KVL];
        if (ti >= 1 && (hf == 1 || i >= 2)) {
          const int g = hf == 0 ? i - 2 : 2 + i;
          const f32x16& x = s[ti - 1];
          if (g < 2) { mx = max3(mx, x[4 * g], x[4 * g + 1]); mx = max3(mx, x[4 * g + 2], x[4 * g + 3]); }
          else mx = max3(mx, x[8 + 2 * (g - 2)], x[9 + 2 * (g - 2)]);
          PIN(mx);
        }
        SBAR();
      }
    }
#pragma unroll
    for (int i = 0; i < 8; ++i) a.k[i] += (ch & 1) ? 65536 : -65536;
    wait1(rfl(early), fl + F_KVL, n + 5);
#pragma unroll
    for (int c = 0; c < 4; ++c) ldv(0, c);
    SBAR();
#pragma unroll
    for (int r = 0; r < 16; r += 2) mx = max3(mx, s[3][r], s[3][r + 1]);
    { auto rr = __builtin_amdgcn_permlane32_swap(__float_as_uint(mx), __float_as_uint(mx), false, false);
      mx = __builtin_fmaxf(__uint_as_float(rr[0]), __uint_as_float(rr[1])); }
    if (ch == 0 || __any(mx > THR)) {
      const float dl = ch == 0 ? mx : __builtin_fmaxf(mx, 0.f);
#pragma unroll
      for (int i = 0; i < 4; ++i)
#pragma unroll
        for (int r = 0; r < 16; ++r) s[i][r] -= dl;
#pragma unroll
      for (int r = 0; r < 16; ++r) negr[r] -= dl;
      if (ch > 0) {
        const float alpha = __builtin_amdgcn_exp2f(-dl);
        ol[0] *= alpha;
#pragma unroll
        for (int c = 0; c < 4; ++c)
#pragma unroll
          for (int r = 0; r < 16; ++r) o[c][r] *= alpha;
      }
    }
    unsigned pw[3][4];
    float pe[2][2];
    auto expo = [&](int step, int i) {
      const f32x16& x = s[step >> 1];
      const int e = 8 * (step & 1) + 2 * i;
      pe[i & 1][0] = __builtin_amdgcn_exp2f(x[e]); pe[i & 1][1] = __builtin_amdgcn_exp2f(x[e + 1]);
      PIN(pe[i & 1][0]); PIN(pe[i & 1][1]);
    };
    auto packw = [&](int step, int i) { pw[step % 3][i] = cvtpk(pe[i & 1][0], pe[i & 1][1]); PIN(pw[step % 3][i]); };
#pragma unroll
    for (int st = 0; st < 2; ++st)
#pragma unroll
      for (int i = 0; i < 4; ++i) { expo(st, i); packw(st, i); }
    SBAR();
#pragma unroll
    for (int step = 0; step < 8; ++step) {
      if (step & 1) { MEMBAR(); prog[0] = n + 4 + (step >> 1) + 1; wait1(rfl(early), fl + F_KVL, (step < 7 || ch < 3) ? n + 4 + (step >> 1) + 2 : 0); }
      SBAR();
      u32x4 pv = {pw[step % 3][0], pw[step % 3][1], pw[step % 3][2], pw[step % 3][3]};
      const bf16x8 pf = __builtin_bit_cast(bf16x8, pv);
#pragma unroll
      for (int c = 0; c < 4; ++c) {
        o[c] = MFMA32(fr[step & 1][c], pf, o[c]);
        if (step + 1 < 8) ldv(step + 1, c);
        else ldk(0, c);
        if (step + 2 < 8) { if (c > 0) packw(step + 2, c - 1); expo(step + 2, c); }
        if (!(step & 1) && c == 0) early = fl[F_KVL];
        SBAR();
      }
      ol = MFMA32(ones, pf, ol);
      if (step + 2 < 8) packw(step + 2, 3);
      SBAR();
    }
#pragma unroll
    for (int i = 0; i < 8; ++i) a.v[i] += (ch & 1) ? 65536 : -65536;
    if (ch == 0 && pend_flag) {
      WAIT_VM(0);
      if (lds_add_ret(fl + F_OC, lane) == 2 && lane == 0) gst(pend_flag, tag);
    }
  }
  const float inv = 1.0f / ol[0];
  wait4(fl + F_KVP, olds_need);
  if (qnext) {
#pragma unroll
    for (int s = 0; s < 8; ++s) qf[s] = *(const LAS bf16x8*)(qnext + s * 1024 + lane * 16);
#pragma unroll
    for (int s = 0; s < 8; ++s) PIN(qf[s]);
    WAIT_LGKM0();
  }
#pragma unroll
  for (int c = 0; c < 4; ++c) {
#pragma unroll
    for (int r = 0; r < 16; ++r) o[c][r] *= inv;
    const bf16x8 f0 = pack8<false>(o[c], 0), f1 = pack8<false>(o[c], 1);
    *(LAS bf16x8*)(olds + (2 * c) * 1024 + lane * 16) = f0;
    *(LAS bf16x8*)(olds + (2 * c + 1) * 1024 + lane * 16) = f1;
    __builtin_amdgcn_raw_buffer_store_b128(__builtin_bit_cast(u32x4, f0), so, ooff + lane * 16 + (2 * c) * 1024, 0, 16);
    __builtin_amdgcn_raw_buffer_store_b128(__builtin_bit_cast(u32x4, f1), so, ooff + lane * 16 + (2 * c + 1) * 1024, 0, 16);
  }
  WAIT_LGKM0();
  lds_add_ret(olds_cnt, lane);
}

__global__ __launch_bounds__(512, 2) void mha_fused(const char* __restrict__ x, const char* __restrict__ kc, const char* __restrict__ vc, const char* __restrict__ wq,
                                                     const char* __restrict__ wo, unsigned short* __restrict__ out, char* __restrict__ ws) {
  extern __shared__ __attribute__((aligned(16))) char lds[];
  const int tid = threadIdx.x, lane = tid & 63, wave = rfl(tid >> 6);
  const int bid = blockIdx.x, xcd = bid & 7, idx = bid >> 3;
  const int hg = xcd >> 1, hl = idx & 3;
  const int b = 8 * (xcd & 1) + (idx >> 2), h0 = 4 * hg + hl;
  unsigned long long* dbg = (unsigned long long*)(ws + (128u << 20)); (void)dbg;
  unsigned* gf = (unsigned*)(ws + WS_F);
  if (wave == 0) STAMP(0);
  lds_flag fl = (lds_flag)(lds + LDS_FLAG);
  const unsigned lds0 = (unsigned)(uintptr_t)(LAS char*)lds;

  const __amdgpu_buffer_rsrc_t sows = __builtin_amdgcn_make_buffer_rsrc((void*)ws, 0, WS_P + WS_P_BYTES, 0x00020000);
  const __amdgpu_buffer_rsrc_t swo = __builtin_amdgcn_make_buffer_rsrc((void*)wo, 0, HD * D * 2, 0x00020000);
  auto head_of = [&](int r, int i) { return 16 * r + 4 * hg + ((i + hl) & 3); };
  unsigned wv[5];
#pragma unroll
  for (int j = 0; j < 5; ++j) { const int pi = 64 * j + lane; wv[j] = (pi / 20) * 1280 + (pi % 20) * 16 - (j < 4 ? 1024 * j : 0); }
  auto issue_w = [&](int h, int buf, int part) {
    const unsigned sb = (unsigned)(h * 128 + 16 * part) * 1280u + hl * 320u, dst = lds0 + buf + part * 5120;
    dma<4, 0>(swo, sb, dst, wv[0], wv[1], wv[2], wv[3]); dma<1, 0>(swo, sb, dst + 4096, wv[4]);
  };

  if (wave == 7) {
    __builtin_amdgcn_s_setprio(3);
    const __amdgpu_buffer_rsrc_t sk = __builtin_amdgcn_make_buffer_rsrc((void*)kc, 0, B * H * SK * DH * 2, 0x00020000);
    const __amdgpu_buffer_rsrc_t sv = __builtin_amdgcn_make_buffer_rsrc((void*)vc, 0, B * H * SK * DH * 2, 0x00020000);
    const unsigned v0 = dma_voff<256>(lane);
    auto kvunit = [&](int u) {
      const int c = u >> 3, head = h0 + 16 * (c >> 2), isv = (u >> 2) & 1;
      const unsigned sbase = (unsigned)((b * H + head) * SK + (c & 3) * 128 + (u & 3) * 32) * 256u;
      const unsigned dst = lds0 + ((c & 1) ? 0 : 65536) + (u & 7) * 8192;
      dma<4, 1>(isv ? sv : sk, sbase, dst, v0, v0 ^ 16, v0 ^ 32, v0 ^ 48);
      dma<4, 1>(isv ? sv : sk, sbase + 4096, dst + 4096, v0, v0 ^ 16, v0 ^ 32, v0 ^ 48);
    };
#pragma unroll 1
    for (int t = 0; t < 10; ++t) RAWBAR();
    unsigned freed = 0;
    int P = 0;
#pragma unroll 1
    for (int u = 0; u < 64; ++u) {
      const unsigned need = u < 8 ? 0 : u < 16 ? 1 : u - 15;
      if (freed < need) {
        freed = min4(fl + F_KVP);
        if (freed < need) {
          if (P < u) { WAIT_VM(0); P = u; fl[F_KVL] = P; }
          wait4(fl + F_KVP, need);
          freed = need;
        }
      }
      kvunit(u);
      if (u == 1 || u == 3) RAWBAR();
      WAIT_VM(24);
      if (P < u - 2) { P = u - 2; fl[F_KVL] = P; }
    }
    WAIT_VM(8); fl[F_KVL] = 63;
    WAIT_VM(0); fl[F_KVL] = 64;
    __builtin_amdgcn_s_setprio(0);
    STAMP(5);
  } else if (wave <= 3) {
    f32x16 acc[2][3] = {};
    qproj((lds_cptr)lds, wave, lane, acc);
#pragma unroll
    for (int d = 0; d < 2; ++d)
#pragma unroll
      for (int qb = 0; qb < 3; ++qb) {
        LAS char* qx = (LAS char*)lds + ((wave >> 1) ? LDS_QB : LDS_Q0) + qb * 8192 + (2 * (2 * (wave & 1) + d)) * 1024 + lane * 16;
        *(LAS bf16x8*)qx = pack8<true>(acc[d][qb], 0);
        *(LAS bf16x8*)(qx + 1024) = pack8<true>(acc[d][qb], 1);
      }
    RAWBAR();
  } else if (wave == 4) {
    fl[lane] = (lane == F_KVP + 3) ? 0x7fffffffu : 0u;
    const unsigned tag0 = gld(gf + FO_EPOCH) + 1u;
    fl[F_EP] = tag0;
    if (lane == 0) gst(gf + FO_SEEN + bid * 16, tag0);
#pragma unroll 1
    for (int t = 0; t < 9; ++t) RAWBAR();
    unsigned wrm = 0;
#pragma unroll
    for (int i = 0; i < 5; ++i) {
      const int L = idx * 320 + i * 64 + lane, hh = L / 1280;
      wrm ^= *(const unsigned*)(wo + (size_t)(16 * (hh >> 2) + 4 * hg + (hh & 3)) * 163840 + (size_t)(L - hh * 1280) * 128);
    }
#pragma unroll 1
    for (int t = 9; t < 12; ++t) RAWBAR();
    asm volatile("" :: "v"(wrm));
    if (bid == 0) {
#pragma unroll
      for (int i = 0; i < 4; ++i) gwait(gf + FO_SEEN + (i * 64 + lane) * 16, tag0);
      if (lane == 0) gst(gf + FO_EPOCH, tag0);
    }
  } else {
    __builtin_amdgcn_s_setprio(3);
    const int pt = wave - 5;
    const int c0 = ((b & 7) + 2 * hl) % 10;
    const __amdgpu_buffer_rsrc_t sw = __builtin_amdgcn_make_buffer_rsrc((void*)wq, 0, D * HD * 2, 0x00020000);
    const __amdgpu_buffer_rsrc_t sx = __builtin_amdgcn_make_buffer_rsrc((void*)x, 0, B * SQ * D * 2, 0x00020000);
    const unsigned v0 = dma_voff<8192>(lane);
    const unsigned xv0 = (lane >> 3) * 1280 + (((lane & 7) ^ (lane >> 4)) * 16);
    auto issue = [&](int t) {
      const int cc = t + c0, c = cc >= 10 ? cc - 10 : cc;
      const unsigned sbase = (unsigned)(c * 64) * 8192u + (h0 + 16 * pt) * 256u;
      const unsigned dst = lds0 + wslot(t);
      const unsigned xs = (unsigned)(b * SQ + 48 * pt) * 1280u + c * 128u;
      dma<4, 0>(sx, xs, dst + 32768 + 6 * pt * 1024, xv0, (xv0 ^ 64) + 9216, xv0 + 2 * 9216, (xv0 ^ 64) + 3 * 9216);
      dma<2, 0>(sx, xs + 4 * 10240, dst + 32768 + (6 * pt + 4) * 1024, xv0, (xv0 ^ 64) + 9216);
#pragma unroll
      for (int j = 0; j < 4; ++j)
        dma<4, 0>(sw, sbase + j * 4 * 32768, dst + pt * 16384 + j * 4096, v0, (v0 ^ 16) + 31744, (v0 ^ 32) + 2 * 31744, (v0 ^ 48) + 3 * 31744);
    };
    issue(0); issue(1);
    WAIT_VM(22); issue(2); RAWBAR();
#pragma unroll 1
    for (int t = 0; t < 10; ++t) {
      if (t < 8) WAIT_VM(22); else WAIT_VM(0);
      RAWBAR();
      if (t + 3 < 10) issue(t + 3);
    }
    RAWBAR();
    __builtin_amdgcn_s_setprio(0);
  }
  const unsigned tag = rfl(fl[F_EP]);

  if (wave <= 2) {
    __builtin_amdgcn_s_setprio(1);
    const int qb = wave;
    bf16x8 qf[8];
    if (wave == 0) STAMP(1);
    AAddr aa; aaddr(aa, (lds_cptr)lds + 65536, lane);
    {
      lds_cptr qx = (lds_cptr)lds + LDS_Q0 + qb * 8192;
#pragma unroll
      for (int s = 0; s < 8; ++s) qf[s] = *(const LAS bf16x8*)(qx + s * 1024 + lane * 16);
#pragma unroll
      for (int s = 0; s < 8; ++s) PIN(qf[s]);
      WAIT_LGKM0();
    }
#pragma unroll 1
    for (int r = 0; r < 2; ++r) {
      attn_head(aa, fl, 32 * r, fl + F_KVP + qb, lane, qf, r == 0 ? (lds_cptr)lds + LDS_QB + qb * 8192 : (lds_cptr) nullptr,
                sows, (unsigned)(((b * H + h0 + 16 * r) * 3 + qb) * 8192), r == 1 ? gf + FO_O + (b * H + h0) * 32 : nullptr, tag,
                (LAS char*)lds + (r ? T_O1 : LDS_QB) + qb * 8192, r ? 56u : 0u, fl + F_OL + r);
      if (wave == 0) STAMP(2 + 2 * r);
    }
    __builtin_amdgcn_s_setprio(0);
  } else {
    if (wave == 3) {
      wait1(0, fl + F_OL, 3);
      gwait(gf + FO_O + (b * H + head_of(0, lane & 3)) * 32, tag);
    } else if (wave < 7) {
      wait4(fl + F_KVP, 56);
#pragma unroll
      for (int p = 0; p < PRE_PARTS; ++p) if (3 * p + (wave - 4) < 8) issue_w(head_of(0, 0), T_W0, 3 * p + (wave - 4));
      WAIT_VM(0);
    }
  }

  {
    const int g = wave & 3, kh = wave >> 2;
    const int hi = lane >> 5, blk = (lane >> 4) & 1, q = (lane & 15) >> 2, p = lane & 3;
    const int aoff = 320 * (64 * kh + 4 * hi + q) + 32 * blk + 8 * p + 64 * g;
    const int gx = g < 3 ? g : 0;
    f32x16 acc[4] = {};
    auto issue_o = [&](int h, int slot) {
      dma<3, 2>(sows, (unsigned)((b * H + h) * 24576 + wave * 3072), lds0 + slot + wave * 3072, lane * 16, lane * 16, lane * 16);
    };
    auto head = [&](int wbuf, int oslot) {
      lds_cptr a0 = (lds_cptr)lds + wbuf + aoff, a1 = a0 + 64 * (4 - g);
      lds_cptr ob = (lds_cptr)lds + oslot + kh * 4096 + lane * 16;
      bf16x8 af[2][2], bf[2][4];
      auto lda = [&](int buf, int k, int i) { lds_cptr o = (i ? a1 : a0) + 320 * 16 * k; af[buf][i] = cat(trd(o), trd(o + 320 * 8)); };
      auto ldb = [&](int buf, int k, int i) { bf[buf][i] = *(const LAS bf16x8*)(ob + (i < 3 ? i : gx) * 8192 + k * 1024); };
      lda(0, 0, 0); ldb(0, 0, 0); ldb(0, 0, 1); ldb(0, 0, 2); lda(0, 0, 1); ldb(0, 0, 3);
#pragma unroll
      for (int k = 0; k < 4; ++k) {
        SBAR();
#pragma unroll
        for (int i = 0; i < 4; ++i) {
          acc[i] = MFMA32(af[k & 1][i == 3], bf[k & 1][i], acc[i]);
          if (k < 3) { ldb((k + 1) & 1, k + 1, i); if (i < 2) lda((k + 1) & 1, k + 1, i); }
          SBAR();
        }
      }
    };
    const int sr[8] = {0, 0, 1, 0, 0, 1, 1, 1}, si[8] = {0, 1, 0, 2, 3, 1, 2, 3};
    const int sl[8] = {LDS_QB, T_OA, T_O1, LDS_QB, T_OA, T_O1, LDS_QB, T_OA};
    RAWBAR();
    if (wave == 0) STAMP(6);
    issue_o(head_of(0, 1), T_OA);
    unsigned pollv = 0;
#pragma unroll
    for (int s = 0; s < 8; ++s) {
      if (s + 1 < 8) issue_w(head_of(sr[s + 1], si[s + 1]), ((s + 1) & 1) ? T_W1 : T_W0, wave);
      if (s >= 1 && s + 2 < 8) issue_o(head_of(sr[s + 2], si[s + 2]), sl[s + 2]);
      if (s == 1 && wave == 3) pollv = gld(gf + FO_O + (b * H + head_of(1, lane & 3)) * 32);
      head((s & 1) ? T_W1 : T_W0, sl[s]);
      if (s == 0 && wave <= 2) {
        WAIT_VM(8);
        if (lds_add_ret(fl + F_OC + 1, lane) == 2 && lane == 0) gst(gf + FO_O + (b * H + h0 + 16) * 32, tag);
      }
      if (s == 2 && wave == 3) {
        if (!__all(pollv == tag)) gwait(gf + FO_O + (b * H + head_of(1, lane & 3)) * 32, tag);
      }
      if (s + 1 < 8) {
        if (s >= 1 && s + 2 < 8) WAIT_VM(3); else WAIT_VM(0);
        RAWBAR();
      }
    }
    if (wave == 0) STAMP(7);
    {
      LAS char* xch = (LAS char*)lds + 65536 + wave * 8192 + lane * 16;
#pragma unroll
      for (int i = 0; i < 2; ++i)
#pragma unroll
        for (int j = 0; j < 4; ++j) { const f32x16& t = acc[kh ? i : 2 + i]; f32x4 v = {t[4 * j], t[4 * j + 1], t[4 * j + 2], t[4 * j + 3]}; *(LAS f32x4*)(xch + (i * 4 + j) * 1024) = v; }
      RAWBAR();
      lds_cptr xr = (lds_cptr)lds + 65536 + (wave ^ 4) * 8192 + lane * 16;
#pragma unroll
      for (int i = 0; i < 2; ++i)
#pragma unroll
        for (int j = 0; j < 4; ++j) { const f32x4 v = *(const LAS f32x4*)(xr + (i * 4 + j) * 1024); f32x16& t = acc[kh ? 2 + i : i];
          t[4 * j] += v[0]; t[4 * j + 1] += v[1]; t[4 * j + 2] += v[2]; t[4 * j + 3] += v[3]; }
    }
    const __amdgpu_buffer_rsrc_t sout = __builtin_amdgcn_make_buffer_rsrc((void*)out, 0, B * SQ * D * 2, 0x00020000);
    const unsigned pbase = WS_P + (unsigned)((b * 4 + hl) * 4) * (15 * 4096);
#pragma unroll
    for (int ii = 0; ii < 2; ++ii) {
      const int i = 2 * kh + ii, tile = i < 3 ? g * 3 + i : 12 + g;
      if ((i < 3 || g < 3) && (tile & 3) != hg) {
        const f32x16& t = kh ? acc[2 + ii] : acc[ii];
#pragma unroll
        for (int j = 0; j < 4; ++j) {
          f32x4 v = {t[4 * j], t[4 * j + 1], t[4 * j + 2], t[4 * j + 3]};
          __builtin_amdgcn_raw_buffer_store_b128(__builtin_bit_cast(u32x4, v), sows, pbase + (hg * 15 + tile) * 4096 + lane * 16 + j * 1024, 0, 16);
        }
      }
    }
    WAIT_VM(0);
    if (lane == 0) gst(gf + FO_P + (((b * 4 + hl) * 4 + hg) * 8 + wave) * 32, tag);
    gwait(gf + FO_P + (((b * 4 + hl) * 4 + ((hg + 1 + (lane % 3)) & 3)) * 8 + wave) * 32, tag);
#pragma unroll
    for (int ii = 0; ii < 2; ++ii) {
      const int i = 2 * kh + ii, tile = i < 3 ? g * 3 + i : 12 + g;
      if ((i < 3 || g < 3) && (tile & 3) == hg) {
        const f32x16& t = kh ? acc[2 + ii] : acc[ii];
        u32x4 pv[3][4];
#pragma unroll
        for (int o = 0; o < 3; ++o)
#pragma unroll
          for (int j = 0; j < 4; ++j) pv[o][j] = __builtin_amdgcn_raw_buffer_load_b128(sows, lane * 16 + j * 1024, pbase + (((hg + 1 + o) & 3) * 15 + tile) * 4096, 16);
        const int nt = i < 3 ? g : 4, qb = i < 3 ? i : g;
        u32x2 w2[4];
#pragma unroll
        for (int j = 0; j < 4; ++j) {
          f32x4 v = {t[4 * j], t[4 * j + 1], t[4 * j + 2], t[4 * j + 3]};
#pragma unroll
          for (int o = 0; o < 3; ++o) v += __builtin_bit_cast(f32x4, pv[o][j]);
          w2[j] = u32x2{cvtpk(v[0], v[1]), cvtpk(v[2], v[3])};
        }
        const unsigned obase = (unsigned)(((b * SQ + 32 * qb + (lane & 31)) * D + 160 * hl + 32 * nt + 8 * hi) * 2);
#pragma unroll
        for (int j = 0; j < 4; j += 2) {
          auto r0 = __builtin_amdgcn_permlane32_swap(w2[j][0], w2[j + 1][0], false, false);
          auto r1 = __builtin_amdgcn_permlane32_swap(w2[j][1], w2[j + 1][1], false, false);
          u32x4 w4 = {r0[0], r1[0], r0[1], r1[1]};
          __builtin_amdgcn_raw_buffer_store_b128(w4, sout, obase + 16 * j, 0, OUT_AUX);
        }
      }
    }
    if (wave == 0) STAMP(8);
  }
}

extern "C" void kernel_launch(void* const* d_in, const int* in_sizes, int n_in, void* d_out, int out_size, void* d_ws, size_t ws_size, hipStream_t stream) {
  (void)hipFuncSetAttribute((const void*)mha_fused, hipFuncAttributeMaxDynamicSharedMemorySize, LDS_BYTES);
  mha_fused<<<256, 512, LDS_BYTES, stream>>>((const char*)d_in[0], (const char*)d_in[1], (const char*)d_in[2], (const char*)d_in[3], (const char*)d_in[4],
                                              (unsigned short*)d_out, (char*)d_ws);
}
```

```cpp
#include <hip/hip_runtime.h>

typedef short bf16x8 __attribute__((ext_vector_type(8)));
typedef short s16x4 __attribute__((ext_vector_type(4)));
typedef float f32x16 __attribute__((ext_vector_type(16)));
typedef float f32x4 __attribute__((ext_vector_type(4)));
typedef float f32x2 __attribute__((ext_vector_type(2)));
typedef unsigned u32x4 __attribute__((ext_vector_type(4)));
typedef unsigned u32x2 __attribute__((ext_vector_type(2)));
typedef __bf16 bf16x2n __attribute__((ext_vector_type(2)));
#define LAS __attribute__((address_space(3)))
typedef LAS const char* lds_cptr;
typedef volatile LAS unsigned* lds_flag;

constexpr int B = 16, SQ = 96, D = 640, H = 32, SK = 512, DH = 128, HD = H * DH;
constexpr int SLOT = 16384;
constexpr int WSLOT = 45056;
constexpr int LDS_Q0 = 0, LDS_QB = 131072  , LDS_FLAG = 155648, LDS_BYTES = LDS_FLAG + 256;
constexpr int T_W0 = 65536, T_W1 = 0, T_O1 = 106496, T_OA = 40960;
constexpr int F_KVL = 0, F_KVP = 4, F_EP = 8, F_OC = 12, F_OL = 16;
constexpr unsigned WS_P = 16u << 20, WS_P_BYTES = 16 * 4 * 4 * 15 * 4096, WS_F = 40u << 20;
constexpr unsigned FO_EPOCH = 0, FO_SEEN = 64, FO_O = FO_SEEN + 256 * 16, FO_P = FO_O + 512 * 32;
constexpr float CS = 0.088388347648318447f * 1.4426950408889634f;
constexpr int PRE_PARTS = 3;
#define GSL 1
#define OUT_AUX 2
constexpr float THR = 8.0f;

#define MFMA32(a, b, c) __builtin_amdgcn_mfma_f32_32x32x16_bf16(a, b, c, 0, 0, 0)
#define SBAR() __builtin_amdgcn_sched_barrier(0)
#define MEMBAR() asm volatile("" ::: "memory")
#define PIN(x) asm volatile("" : "+v"(x))
#define DI __device__ __forceinline__
#define WAIT_VM(n) asm volatile("s_waitcnt vmcnt(" #n ")" ::: "memory")
#define WAIT_LGKM0() asm volatile("s_waitcnt lgkmcnt(0)" ::: "memory")
#define RAWBAR() do { WAIT_LGKM0(); __builtin_amdgcn_s_barrier(); MEMBAR(); } while (0)

DI unsigned cvtpk(float lo, float hi) { f32x2 v = {lo, hi}; return __builtin_bit_cast(unsigned, __builtin_convertvector(v, bf16x2n)); }
DI s16x4 trd(lds_cptr p) { return __builtin_amdgcn_ds_read_tr16_b64_v4i16((LAS s16x4*)p); }
DI bf16x8 cat(s16x4 a, s16x4 b) { return __builtin_shufflevector(a, b, 0, 1, 2, 3, 4, 5, 6, 7); }
template <bool SCALE> DI bf16x8 pack8(const f32x16& x, int s) {
  const float c = SCALE ? CS : 1.0f;
  u32x4 w = {cvtpk(x[8 * s] * c, x[8 * s + 1] * c), cvtpk(x[8 * s + 2] * c, x[8 * s + 3] * c), cvtpk(x[8 * s + 4] * c, x[8 * s + 5] * c), cvtpk(x[8 * s + 6] * c, x[8 * s + 7] * c)};
  return __builtin_bit_cast(bf16x8, w);
}
DI unsigned rfl(unsigned v) { return __builtin_amdgcn_readfirstlane(v); }
DI unsigned lds_addr(lds_flag f) { return (unsigned)(uintptr_t)f; }

DI void wait1(unsigned early, lds_flag f, unsigned need) {
  unsigned t, sv, cnt;
  asm volatile("s_cmp_ge_u32 %3, %4\n\ts_cbranch_scc1 W1E%=\n\ts_mov_b32 %2, 0\n"
               "W1L%=:\n\tds_read_b32 %0, %5\n\ts_waitcnt lgkmcnt(0)\n\tv_readfirstlane_b32 %1, %0\n\ts_cmp_ge_u32 %1, %4\n\ts_cbranch_scc1 W1E%=\n\t"
               "s_sleep 1\n\ts_add_u32 %2, %2, 1\n\ts_cmp_lt_u32 %2, 0x40000\n\ts_cbranch_scc1 W1L%=\n"
               "W1E%=:"
               : "=&v"(t), "=&s"(sv), "=&s"(cnt) : "s"(early), "s"(need), "v"(lds_addr(f)) : "memory", "scc");
}
DI void wait4(lds_flag f, unsigned need) {
  unsigned a0, a1, a2, m, sv, cnt;
  asm volatile("s_mov_b32 %5, 0\n"
               "W4L%=:\n\tds_read_b32 %3, %7\n\tds_read_b32 %0, %7 offset:4\n\tds_read_b32 %1, %7 offset:8\n\tds_read_b32 %2, %7 offset:12\n\ts_waitcnt lgkmcnt(0)\n\t"
               "v_min_u32 %3, %3, %0\n\tv_min3_u32 %3, %3, %1, %2\n\ts_nop 0\n\tv_readfirstlane_b32 %4, %3\n\ts_cmp_ge_u32 %4, %6\n\ts_cbranch_scc1 W4E%=\n\t"
               "s_sleep 1\n\ts_add_u32 %5, %5, 1\n\ts_cmp_lt_u32 %5, 0x40000\n\ts_cbranch_scc1 W4L%=\n"
               "W4E%=:"
               : "=&v"(a0), "=&v"(a1), "=&v"(a2), "=&v"(m), "=&s"(sv), "=&s"(cnt) : "s"(need), "v"(lds_addr(f)) : "memory", "scc");
}
DI unsigned min4(lds_flag f) { u32x4 v = *(volatile LAS u32x4*)f; unsigned m = v[0] < v[1] ? v[0] : v[1]; m = m < v[2] ? m : v[2]; return rfl(m < v[3] ? m : v[3]); }
DI unsigned lds_add_ret(lds_flag f, int lane) {
  unsigned old = 0;
  if (lane == 0) old = __hip_atomic_fetch_add((LAS unsigned*)f, 1u, __ATOMIC_RELAXED, __HIP_MEMORY_SCOPE_WORKGROUP);
  return rfl(old);
}
DI unsigned gld(const unsigned* p) { return __hip_atomic_load(p, __ATOMIC_RELAXED, __HIP_MEMORY_SCOPE_AGENT); }
DI void gst(unsigned* p, unsigned v) { __hip_atomic_store(p, v, __ATOMIC_RELAXED, __HIP_MEMORY_SCOPE_AGENT); }
DI void gwait(const unsigned* p, unsigned tag) {
  for (unsigned spins = 0; spins < (1u << 15); ++spins) {
    if (__all(gld(p) == tag)) break;
    __builtin_amdgcn_s_sleep(GSL);
  }
  MEMBAR();
}

#define DMA_HEAD "s_nop 2\n\ts_mov_b32 m0, %0\n\ts_nop 0\n\t"
#define DMA_BODY(POL) \
  if constexpr (N == 1) asm volatile(DMA_HEAD "buffer_load_dwordx4 %1, %2, %3 offen" POL " lds" ::"s"(dst), "v"(va), "s"(srd), "s"(soff) : "m0", "memory"); \
  else if constexpr (N == 2) asm volatile(DMA_HEAD "buffer_load_dwordx4 %1, %3, %4 offen" POL " lds\n\tbuffer_load_dwordx4 %2, %3, %4 offen offset:1024" POL " lds" \
                                          ::"s"(dst), "v"(va), "v"(vb), "s"(srd), "s"(soff) : "m0", "memory"); \
  else if constexpr (N == 3) asm volatile(DMA_HEAD "buffer_load_dwordx4 %1, %4, %5 offen" POL " lds\n\tbuffer_load_dwordx4 %2, %4, %5 offen offset:1024" POL " lds\n\t" \
                                          "buffer_load_dwordx4 %3, %4, %5 offen offset:2048" POL " lds" ::"s"(dst), "v"(va), "v"(vb), "v"(vc), "s"(srd), "s"(soff) : "m0", "memory"); \
  else asm volatile(DMA_HEAD "buffer_load_dwordx4 %1, %5, %6 offen" POL " lds\n\tbuffer_load_dwordx4 %2, %5, %6 offen offset:1024" POL " lds\n\t" \
                    "buffer_load_dwordx4 %3, %5, %6 offen offset:2048" POL " lds\n\tbuffer_load_dwordx4 %4, %5, %6 offen offset:3072" POL " lds" \
                    ::"s"(dst), "v"(va), "v"(vb), "v"(vc), "v"(vd), "s"(srd), "s"(soff) : "m0", "memory");
template <int N, int POL>
DI void dma(__amdgpu_buffer_rsrc_t srd, unsigned soff, unsigned dst, unsigned va, unsigned vb = 0, unsigned vc = 0, unsigned vd = 0) {
  dst = rfl(dst); soff = rfl(soff);
  if constexpr (POL == 0) { DMA_BODY("") } else if constexpr (POL == 1) { DMA_BODY(" nt") } else { DMA_BODY(" sc1") }
}
template <int PITCH> DI unsigned dma_voff(int i) { return (i >> 4) * PITCH + 16 * ((i & 15) ^ ((i >> 4) << 2)); }

DI int wslot(int t) { return (t % 3) * WSLOT; }
DI void qproj(lds_cptr lds, int i, int lane, f32x16 (&acc)[2][3]) {
  const int hi = lane >> 5, blk = (lane >> 4) & 1, q = (lane & 15) >> 2, pp = lane & 3, r32 = lane & 31;
  lds_cptr aw[4], ax[4];
#pragma unroll
  for (int d = 0; d < 2; ++d)
#pragma unroll
    for (int t = 0; t < 2; ++t)
      aw[2 * d + t] = lds + 16384 * (i >> 1) + 2048 * hi + 1024 * t + 256 * q + 16 * (4 * ((2 * (i & 1) + d) ^ q) + ((2 * blk + (pp & 1)) ^ (2 * hi + t))) + 8 * (pp >> 1);
#pragma unroll
  for (int u = 0; u < 4; ++u) ax[u] = lds + 32768 + 128 * r32 + 16 * ((2 * u + hi) ^ ((r32 >> 1) & 7));
  bf16x8 wf[2][2], xf[2][3];
  auto ldw = [&](int buf, int so, int u, int d) { wf[buf][d] = cat(trd(aw[2 * d] + so + 4096 * u), trd(aw[2 * d + 1] + so + 4096 * u)); };
  auto ldx = [&](int buf, int so, int u, int qb) { xf[buf][qb] = *(const LAS bf16x8*)(ax[u] + so + 4096 * qb); };
  RAWBAR();
  int so = wslot(0);
#pragma unroll
  for (int d = 0; d < 2; ++d) ldw(0, so, 0, d);
#pragma unroll
  for (int qb = 0; qb < 3; ++qb) ldx(0, so, 0, qb);
#pragma unroll 1
  for (int t = 0; t < 10; ++t) {
    const int son = wslot(t + 1);
#pragma unroll
    for (int u = 0; u < 4; ++u) {
      SBAR();
      const int nb = (u + 1) & 1, nso = u < 3 ? so : son, nu = (u + 1) & 3;
#pragma unroll
      for (int g = 0; g < 6; ++g) {
        const int d = g / 3, qb = g % 3;
        acc[d][qb] = MFMA32(wf[u & 1][d], xf[u & 1][qb], acc[d][qb]);
        if (u < 3) {
          if (g == 0) ldw(nb, nso, nu, 0);
          else if (g < 4) ldx(nb, nso, nu, g - 1);
          else if (g == 4) ldw(nb, nso, nu, 1);
        } else {
          if (g == 2) { SBAR(); RAWBAR(); ldw(nb, nso, nu, 0); ldx(nb, nso, nu, 0); }
          else if (g == 3) { ldx(nb, nso, nu, 1); ldx(nb, nso, nu, 2); }
          else if (g == 4) ldw(nb, nso, nu, 1);
        }
        SBAR();
      }
    }
    so = son;
  }
}

struct AAddr { lds_cptr k[8]; lds_cptr v[8]; };
DI void aaddr(AAddr& a, lds_cptr kv, int lane) {
  const int r32 = lane & 31, hi = lane >> 5, blk = (lane >> 4) & 1, q = (lane & 15) >> 2, p = lane & 3;
  const int f = ((r32 & 3) << 2) | ((r32 >> 2) & 3);
#pragma unroll
  for (int s = 0; s < 8; ++s) a.k[s] = kv + 256 * r32 + 16 * ((2 * s + hi) ^ f);
#pragma unroll
  for (int c = 0; c < 4; ++c)
#pragma unroll
    for (int t = 0; t < 2; ++t)
      a.v[2 * c + t] = kv + 2 * SLOT + 2048 * t + 1024 * hi + 256 * q + 16 * (4 * (c ^ q) + ((2 * blk + (p >> 1)) ^ (2 * t + hi))) + 8 * (p & 1);
}
DI float max3(float a, float b, float c) { return __builtin_fmaxf(__builtin_fmaxf(a, b), c); }
DI void attn_head(AAddr& a, lds_flag fl, int n0  , lds_flag prog, int lane, bf16x8 (&qf)[8], lds_cptr qnext,
                  __amdgpu_buffer_rsrc_t so, unsigned ooff, unsigned* pend_flag, unsigned tag, LAS char* olds  , unsigned olds_need, lds_flag olds_cnt) {
  f32x16 o[4] = {};
  f32x16 ol = {};
  f32x16 negr = {};
  const bf16x8 ones = {0x3F80, 0x3F80, 0x3F80, 0x3F80, 0x3F80, 0x3F80, 0x3F80, 0x3F80};
  bf16x8 fr[2][4];
  auto ldk = [&](int step, int i) {
    const int tt = step >> 2, kb = (step >> 1) & 1, hf = step & 1;
    fr[step & 1][i] = *(const LAS bf16x8*)(a.k[4 * hf + i] + SLOT * tt + 8192 * kb);
  };
  auto ldv = [&](int step, int c) {
    const int tt = step >> 2, ks = step & 3;
    fr[step & 1][c] = cat(trd(a.v[2 * c] + SLOT * tt + 4096 * ks), trd(a.v[2 * c + 1] + SLOT * tt + 4096 * ks));
  };
  wait1(0, fl + F_KVL, n0 + 1);
#pragma unroll
  for (int i = 0; i < 4; ++i) ldk(0, i);
#pragma unroll 1
  for (int ch = 0; ch < 4; ++ch) {
    const int n = n0 + 8 * ch;
    f32x16 s[4];
    unsigned early = 0;
    float mx = -__builtin_inff();
#pragma unroll
    for (int step = 0; step < 8; ++step) {
      if (step & 1) { MEMBAR(); prog[0] = n + (step >> 1) + 1; if (step < 7) wait1(rfl(early), fl + F_KVL, n + (step >> 1) + 2); }
      SBAR();
      const int ti = step >> 1, hf = step & 1;
#pragma unroll
      for (int i = 0; i < 4; ++i) {
        if (hf == 0 && i == 0) s[ti] = MFMA32(fr[step & 1][0], qf[0], negr);
        else s[ti] = MFMA32(fr[step & 1][i], qf[4 * hf + i], s[ti]);
        if (step + 1 < 8) ldk(step + 1, i);
        if ((!(step & 1) || step == 7) && i == 0) early = fl[F_KVL];
        if (ti >= 1 && (hf == 1 || i >= 2)) {
          const int g = hf == 0 ? i - 2 : 2 + i;
          const f32x16& x = s[ti - 1];
          if (g < 2) { mx = max3(mx, x[4 * g], x[4 * g + 1]); mx = max3(mx, x[4 * g + 2], x[4 * g + 3]); }
          else mx = max3(mx, x[8 + 2 * (g - 2)], x[9 + 2 * (g - 2)]);
          PIN(mx);
        }
        SBAR();
      }
    }
#pragma unroll
    for (int i = 0; i < 8; ++i) a.k[i] += (ch & 1) ? 65536 : -65536;
    wait1(rfl(early), fl + F_KVL, n + 5);
#pragma unroll
    for (int c = 0; c < 4; ++c) ldv(0, c);
    SBAR();
#pragma unroll
    for (int r = 0; r < 16; r += 2) mx = max3(mx, s[3][r], s[3][r + 1]);
    { auto rr = __builtin_amdgcn_permlane32_swap(__float_as_uint(mx), __float_as_uint(mx), false, false);
      mx = __builtin_fmaxf(__uint_as_float(rr[0]), __uint_as_float(rr[1])); }
    if (ch == 0 || __any(mx > THR)) {
      const float dl = ch == 0 ? mx : __builtin_fmaxf(mx, 0.f);
#pragma unroll
      for (int i = 0; i < 4; ++i)
#pragma unroll
        for (int r = 0; r < 16; ++r) s[i][r] -= dl;
#pragma unroll
      for (int r = 0; r < 16; ++r) negr[r] -= dl;
      if (ch > 0) {
        const float alpha = __builtin_amdgcn_exp2f(-dl);
        ol[0] *= alpha;
#pragma unroll
        for (int c = 0; c < 4; ++c)
#pragma unroll
          for (int r = 0; r < 16; ++r) o[c][r] *= alpha;
      }
    }
    unsigned pw[3][4];
    float pe[2][2];
    auto expo = [&](int step, int i) {
      const f32x16& x = s[step >> 1];
      const int e = 8 * (step & 1) + 2 * i;
      pe[i & 1][0] = __builtin_amdgcn_exp2f(x[e]); pe[i & 1][1] = __builtin_amdgcn_exp2f(x[e + 1]);
      PIN(pe[i & 1][0]); PIN(pe[i & 1][1]);
    };
    auto packw = [&](int step, int i) { pw[step % 3][i] = cvtpk(pe[i & 1][0], pe[i & 1][1]); PIN(pw[step % 3][i]); };
#pragma unroll
    for (int st = 0; st < 2; ++st)
#pragma unroll
      for (int i = 0; i < 4; ++i) { expo(st, i); packw(st, i); }
    SBAR();
#pragma unroll
    for (int step = 0; step < 8; ++step) {
      if (step & 1) { MEMBAR(); prog[0] = n + 4 + (step >> 1) + 1; wait1(rfl(early), fl + F_KVL, (step < 7 || ch < 3) ? n + 4 + (step >> 1) + 2 : 0); }
      SBAR();
      u32x4 pv = {pw[step % 3][0], pw[step % 3][1], pw[step % 3][2], pw[step % 3][3]};
      const bf16x8 pf = __builtin_bit_cast(bf16x8, pv);
#pragma unroll
      for (int c = 0; c < 4; ++c) {
        o[c] = MFMA32(fr[step & 1][c], pf, o[c]);
        if (step + 1 < 8) ldv(step + 1, c);
        else ldk(0, c);
        if (step + 2 < 8) { if (c > 0) packw(step + 2, c - 1); expo(step + 2, c); }
        if (!(step & 1) && c == 0) early = fl[F_KVL];
        SBAR();
      }
      ol = MFMA32(ones, pf, ol);
      if (step + 2 < 8) packw(step + 2, 3);
      SBAR();
    }
#pragma unroll
    for (int i = 0; i < 8; ++i) a.v[i] += (ch & 1) ? 65536 : -65536;
    if (ch == 0 && pend_flag) {
      WAIT_VM(0);
      if (lds_add_ret(fl + F_OC, lane) == 2 && lane == 0) gst(pend_flag, tag);
    }
  }
  const float inv = __builtin_amdgcn_rcpf(ol[0]);
  wait4(fl + F_KVP, olds_need);
  if (qnext) {
#pragma unroll
    for (int s = 0; s < 8; ++s) qf[s] = *(const LAS bf16x8*)(qnext + s * 1024 + lane * 16);
#pragma unroll
    for (int s = 0; s < 8; ++s) PIN(qf[s]);
    WAIT_LGKM0();
  }
#pragma unroll
  for (int c = 0; c < 4; ++c) {
#pragma unroll
    for (int r = 0; r < 16; ++r) o[c][r] *= inv;
    const bf16x8 f0 = pack8<false>(o[c], 0), f1 = pack8<false>(o[c], 1);
    *(LAS bf16x8*)(olds + (2 * c) * 1024 + lane * 16) = f0;
    *(LAS bf16x8*)(olds + (2 * c + 1) * 1024 + lane * 16) = f1;
    __builtin_amdgcn_raw_buffer_store_b128(__builtin_bit_cast(u32x4, f0), so, ooff + lane * 16 + (2 * c) * 1024, 0, 16);
    __builtin_amdgcn_raw_buffer_store_b128(__builtin_bit_cast(u32x4, f1), so, ooff + lane * 16 + (2 * c + 1) * 1024, 0, 16);
  }
  WAIT_LGKM0();
  lds_add_ret(olds_cnt, lane);
}

__global__ __launch_bounds__(512, 2) void mha_fused(const char* __restrict__ x, const char* __restrict__ kc, const char* __restrict__ vc, const char* __restrict__ wq,
                                                     const char* __restrict__ wo, unsigned short* __restrict__ out, char* __restrict__ ws) {
  extern __shared__ __attribute__((aligned(16))) char lds[];
  const int tid = threadIdx.x, lane = tid & 63, wave = rfl(tid >> 6);
  const int bid = blockIdx.x, xcd = bid & 7, idx = bid >> 3;
  const int hg = xcd >> 1, hl = idx & 3;
  const int b = 8 * (xcd & 1) + (idx >> 2), h0 = 4 * hg + hl;
  unsigned* gf = (unsigned*)(ws + WS_F);
  lds_flag fl = (lds_flag)(lds + LDS_FLAG);
  const unsigned lds0 = (unsigned)(uintptr_t)(LAS char*)lds;

  const __amdgpu_buffer_rsrc_t sows = __builtin_amdgcn_make_buffer_rsrc((void*)ws, 0, WS_P + WS_P_BYTES, 0x00020000);
  const __amdgpu_buffer_rsrc_t swo = __builtin_amdgcn_make_buffer_rsrc((void*)wo, 0, HD * D * 2, 0x00020000);
  auto head_of = [&](int r, int i) { return 16 * r + 4 * hg + ((i + hl) & 3); };
  unsigned wv[5];
#pragma unroll
  for (int j = 0; j < 5; ++j) { const int pi = 64 * j + lane; wv[j] = (pi / 20) * 1280 + (pi % 20) * 16 - (j < 4 ? 1024 * j : 0); }
  auto issue_w = [&](int h, int buf, int part) {
    const unsigned sb = (unsigned)(h * 128 + 16 * part) * 1280u + hl * 320u, dst = lds0 + buf + part * 5120;
    dma<4, 0>(swo, sb, dst, wv[0], wv[1], wv[2], wv[3]); dma<1, 0>(swo, sb, dst + 4096, wv[4]);
  };

  if (wave == 7) {
    __builtin_amdgcn_s_setprio(3);
    const __amdgpu_buffer_rsrc_t sk = __builtin_amdgcn_make_buffer_rsrc((void*)kc, 0, B * H * SK * DH * 2, 0x00020000);
    const __amdgpu_buffer_rsrc_t sv = __builtin_amdgcn_make_buffer_rsrc((void*)vc, 0, B * H * SK * DH * 2, 0x00020000);
    const unsigned v0 = dma_voff<256>(lane);
    auto kvunit = [&](int u) {
      const int c = u >> 3, head = h0 + 16 * (c >> 2), isv = (u >> 2) & 1;
      const unsigned sbase = (unsigned)((b * H + head) * SK + (c & 3) * 128 + (u & 3) * 32) * 256u;
      const unsigned dst = lds0 + ((c & 1) ? 0 : 65536) + (u & 7) * 8192;
      dma<4, 1>(isv ? sv : sk, sbase, dst, v0, v0 ^ 16, v0 ^ 32, v0 ^ 48);
      dma<4, 1>(isv ? sv : sk, sbase + 4096, dst + 4096, v0, v0 ^ 16, v0 ^ 32, v0 ^ 48);
    };
#pragma unroll 1
    for (int t = 0; t < 9; ++t) RAWBAR();
    unsigned freed = 0;
    int P = 0;
#pragma unroll 1
    for (int u = 0; u < 64; ++u) {
      const unsigned need = u < 8 ? 0 : u < 16 ? 1 : u - 15;
      if (freed < need) {
        freed = min4(fl + F_KVP);
        if (freed < need) {
          if (P < u) { WAIT_VM(0); P = u; fl[F_KVL] = P; }
          wait4(fl + F_KVP, need);
          freed = need;
        }
      }
      if (u == 3) RAWBAR();
      kvunit(u);
      if (u == 3 || u == 4) RAWBAR();
      WAIT_VM(24);
      if (P < u - 2) { P = u - 2; fl[F_KVL] = P; }
    }
    WAIT_VM(8); fl[F_KVL] = 63;
    WAIT_VM(0); fl[F_KVL] = 64;
    __builtin_amdgcn_s_setprio(0);
  } else if (wave <= 3) {
    f32x16 acc[2][3] = {};
    qproj((lds_cptr)lds, wave, lane, acc);
#pragma unroll
    for (int d = 0; d < 2; ++d)
#pragma unroll
      for (int qb = 0; qb < 3; ++qb) {
        LAS char* qx = (LAS char*)lds + ((wave >> 1) ? LDS_QB : LDS_Q0) + qb * 8192 + (2 * (2 * (wave & 1) + d)) * 1024 + lane * 16;
        *(LAS bf16x8*)qx = pack8<true>(acc[d][qb], 0);
        *(LAS bf16x8*)(qx + 1024) = pack8<true>(acc[d][qb], 1);
      }
    RAWBAR();
  } else if (wave == 4) {
    fl[lane] = (lane == F_KVP + 3) ? 0x7fffffffu : 0u;
    const unsigned tag0 = gld(gf + FO_EPOCH) + 1u;
    fl[F_EP] = tag0;
    if (lane == 0) gst(gf + FO_SEEN + bid * 16, tag0);
#pragma unroll 1
    for (int t = 0; t < 9; ++t) RAWBAR();
    unsigned wrm = 0;
#pragma unroll
    for (int i = 0; i < 5; ++i) {
      const int L = idx * 320 + i * 64 + lane, hh = L / 1280;
      wrm ^= *(const unsigned*)(wo + (size_t)(16 * (hh >> 2) + 4 * hg + (hh & 3)) * 163840 + (size_t)(L - hh * 1280) * 128);
    }
#pragma unroll 1
    for (int t = 9; t < 12; ++t) RAWBAR();
    asm volatile("" :: "v"(wrm));
    if (bid == 0) {
#pragma unroll
      for (int i = 0; i < 4; ++i) gwait(gf + FO_SEEN + (i * 64 + lane) * 16, tag0);
      if (lane == 0) gst(gf + FO_EPOCH, tag0);
    }
  } else {
    __builtin_amdgcn_s_setprio(3);
    const int pt = wave - 5;
    const int c0 = ((b & 7) + 2 * hl) % 10;
    const __amdgpu_buffer_rsrc_t sw = __builtin_amdgcn_make_buffer_rsrc((void*)wq, 0, D * HD * 2, 0x00020000);
    const __amdgpu_buffer_rsrc_t sx = __builtin_amdgcn_make_buffer_rsrc((void*)x, 0, B * SQ * D * 2, 0x00020000);
    const unsigned v0 = dma_voff<8192>(lane);
    const unsigned xv0 = (lane >> 3) * 1280 + (((lane & 7) ^ (lane >> 4)) * 16);
    auto issue = [&](int t) {
      const int cc = t + c0, c = cc >= 10 ? cc - 10 : cc;
      const unsigned sbase = (unsigned)(c * 64) * 8192u + (h0 + 16 * pt) * 256u;
      const unsigned dst = lds0 + wslot(t);
      const unsigned xs = (unsigned)(b * SQ + 48 * pt) * 1280u + c * 128u;
      dma<4, 0>(sx, xs, dst + 32768 + 6 * pt * 1024, xv0, (xv0 ^ 64) + 9216, xv0 + 2 * 9216, (xv0 ^ 64) + 3 * 9216);
      dma<2, 0>(sx, xs + 4 * 10240, dst + 32768 + (6 * pt + 4) * 1024, xv0, (xv0 ^ 64) + 9216);
#pragma unroll
      for (int j = 0; j < 4; ++j)
        dma<4, 0>(sw, sbase + j * 4 * 32768, dst + pt * 16384 + j * 4096, v0, (v0 ^ 16) + 31744, (v0 ^ 32) + 2 * 31744, (v0 ^ 48) + 3 * 31744);
    };
    issue(0); issue(1);
    WAIT_VM(22); issue(2); RAWBAR();
#pragma unroll 1
    for (int t = 0; t < 10; ++t) {
      if (t < 8) WAIT_VM(22); else WAIT_VM(0);
      RAWBAR();
      if (t + 3 < 10) issue(t + 3);
    }
    RAWBAR();
    __builtin_amdgcn_s_setprio(0);
  }
  const unsigned tag = rfl(fl[F_EP]);

  if (wave <= 2) {
    __builtin_amdgcn_s_setprio(1);
    const int qb = wave;
    bf16x8 qf[8];
    AAddr aa; aaddr(aa, (lds_cptr)lds + 65536, lane);
    {
      lds_cptr qx = (lds_cptr)lds + LDS_Q0 + qb * 8192;
#pragma unroll
      for (int s = 0; s < 8; ++s) qf[s] = *(const LAS bf16x8*)(qx + s * 1024 + lane * 16);
#pragma unroll
      for (int s = 0; s < 8; ++s) PIN(qf[s]);
      WAIT_LGKM0();
    }
#pragma unroll 1
    for (int r = 0; r < 2; ++r) {
      attn_head(aa, fl, 32 * r, fl + F_KVP + qb, lane, qf, r == 0 ? (lds_cptr)lds + LDS_QB + qb * 8192 : (lds_cptr) nullptr,
                sows, (unsigned)(((b * H + h0 + 16 * r) * 3 + qb) * 8192), r == 1 ? gf + FO_O + (b * H + h0) * 32 : nullptr, tag,
                (LAS char*)lds + (r ? T_O1 : LDS_QB) + qb * 8192, r ? 56u : 0u, fl + F_OL + r);
    }
    __builtin_amdgcn_s_setprio(0);
  } else {
    if (wave == 3) {
      wait1(0, fl + F_OL, 3);
      gwait(gf + FO_O + (b * H + head_of(0, lane & 3)) * 32, tag);
    } else if (wave < 7) {
      wait4(fl + F_KVP, 56);
#pragma unroll
      for (int p = 0; p < PRE_PARTS; ++p) if (3 * p + (wave - 4) < 8) issue_w(head_of(0, 0), T_W0, 3 * p + (wave - 4));
      WAIT_VM(0);
    }
  }

  {
    const int g = wave & 3, kh = wave >> 2;
    const int hi = lane >> 5, blk = (lane >> 4) & 1, q = (lane & 15) >> 2, p = lane & 3;
    const int aoff = 320 * (64 * kh + 4 * hi + q) + 32 * blk + 8 * p + 64 * g;
    const int gx = g < 3 ? g : 0;
    f32x16 acc[4] = {};
    auto issue_o = [&](int h, int slot) {
      dma<3, 2>(sows, (unsigned)((b * H + h) * 24576 + wave * 3072), lds0 + slot + wave * 3072, lane * 16, lane * 16, lane * 16);
    };
    auto head = [&](int wbuf, int oslot) {
      lds_cptr a0 = (lds_cptr)lds + wbuf + aoff, a1 = a0 + 64 * (4 - g);
      lds_cptr ob = (lds_cptr)lds + oslot + kh * 4096 + lane * 16;
      bf16x8 af[2][2], bf[2][4];
      auto lda = [&](int buf, int k, int i) { lds_cptr o = (i ? a1 : a0) + 320 * 16 * k; af[buf][i] = cat(trd(o), trd(o + 320 * 8)); };
      auto ldb = [&](int buf, int k, int i) { bf[buf][i] = *(const LAS bf16x8*)(ob + (i < 3 ? i : gx) * 8192 + k * 1024); };
      lda(0, 0, 0); ldb(0, 0, 0); ldb(0, 0, 1); ldb(0, 0, 2); lda(0, 0, 1); ldb(0, 0, 3);
#pragma unroll
      for (int k = 0; k < 4; ++k) {
        SBAR();
#pragma unroll
        for (int i = 0; i < 4; ++i) {
          acc[i] = MFMA32(af[k & 1][i == 3], bf[k & 1][i], acc[i]);
          if (k < 3) { ldb((k + 1) & 1, k + 1, i); if (i < 2) lda((k + 1) & 1, k + 1, i); }
          SBAR();
        }
      }
    };
    const int sr[8] = {0, 0, 1, 0, 0, 1, 1, 1}, si[8] = {0, 1, 0, 2, 3, 1, 2, 3};
    const int sl[8] = {LDS_QB, T_OA, T_O1, LDS_QB, T_OA, T_O1, LDS_QB, T_OA};
    RAWBAR();
    issue_o(head_of(0, 1), T_OA);
    unsigned pollv = 0;
#pragma unroll
    for (int s = 0; s < 8; ++s) {
      if (s + 1 < 8) issue_w(head_of(sr[s + 1], si[s + 1]), ((s + 1) & 1) ? T_W1 : T_W0, wave);
      if (s >= 1 && s + 2 < 8) issue_o(head_of(sr[s + 2], si[s + 2]), sl[s + 2]);
      if (s == 1 && wave == 3) pollv = gld(gf + FO_O + (b * H + head_of(1, lane & 3)) * 32);
      head((s & 1) ? T_W1 : T_W0, sl[s]);
      if (s == 0 && wave <= 2) {
        WAIT_VM(8);
        if (lds_add_ret(fl + F_OC + 1, lane) == 2 && lane == 0) gst(gf + FO_O + (b * H + h0 + 16) * 32, tag);
      }
      if (s == 2 && wave == 3) {
        if (!__all(pollv == tag)) gwait(gf + FO_O + (b * H + head_of(1, lane & 3)) * 32, tag);
      }
      if (s + 1 < 8) {
        if (s >= 1 && s + 2 < 8) WAIT_VM(3); else WAIT_VM(0);
        RAWBAR();
      }
    }
    {
      LAS char* xch = (LAS char*)lds + 65536 + wave * 8192 + lane * 16;
#pragma unroll
      for (int i = 0; i < 2; ++i)
#pragma unroll
        for (int j = 0; j < 4; ++j) { const f32x16& t = acc[kh ? i : 2 + i]; f32x4 v = {t[4 * j], t[4 * j + 1], t[4 * j + 2], t[4 * j + 3]}; *(LAS f32x4*)(xch + (i * 4 + j) * 1024) = v; }
      RAWBAR();
      lds_cptr xr = (lds_cptr)lds + 65536 + (wave ^ 4) * 8192 + lane * 16;
#pragma unroll
      for (int i = 0; i < 2; ++i)
#pragma unroll
        for (int j = 0; j < 4; ++j) { const f32x4 v = *(const LAS f32x4*)(xr + (i * 4 + j) * 1024); f32x16& t = acc[kh ? 2 + i : i];
          t[4 * j] += v[0]; t[4 * j + 1] += v[1]; t[4 * j + 2] += v[2]; t[4 * j + 3] += v[3]; }
    }
    const __amdgpu_buffer_rsrc_t sout = __builtin_amdgcn_make_buffer_rsrc((void*)out, 0, B * SQ * D * 2, 0x00020000);
    const unsigned pbase = WS_P + (unsigned)((b * 4 + hl) * 4) * (15 * 4096);
#pragma unroll
    for (int ii = 0; ii < 2; ++ii) {
      const int i = 2 * kh + ii, tile = i < 3 ? g * 3 + i : 12 + g;
      if ((i < 3 || g < 3) && (tile & 3) != hg) {
        const f32x16& t = kh ? acc[2 + ii] : acc[ii];
#pragma unroll
        for (int j = 0; j < 4; ++j) {
          f32x4 v = {t[4 * j], t[4 * j + 1], t[4 * j + 2], t[4 * j + 3]};
          __builtin_amdgcn_raw_buffer_store_b128(__builtin_bit_cast(u32x4, v), sows, pbase + (hg * 15 + tile) * 4096 + lane * 16 + j * 1024, 0, 16);
        }
      }
    }
    WAIT_VM(0);
    if (lane == 0) gst(gf + FO_P + (((b * 4 + hl) * 4 + hg) * 8 + wave) * 32, tag);
    gwait(gf + FO_P + (((b * 4 + hl) * 4 + ((hg + 1 + (lane % 3)) & 3)) * 8 + wave) * 32, tag);
#pragma unroll
    for (int ii = 0; ii < 2; ++ii) {
      const int i = 2 * kh + ii, tile = i < 3 ? g * 3 + i : 12 + g;
      if ((i < 3 || g < 3) && (tile & 3) == hg) {
        const f32x16& t = kh ? acc[2 + ii] : acc[ii];
        u32x4 pv[3][4];
#pragma unroll
        for (int o = 0; o < 3; ++o)
#pragma unroll
          for (int j = 0; j < 4; ++j) pv[o][j] = __builtin_amdgcn_raw_buffer_load_b128(sows, lane * 16 + j * 1024, pbase + (((hg + 1 + o) & 3) * 15 + tile) * 4096, 16);
        const int nt = i < 3 ? g : 4, qb = i < 3 ? i : g;
        u32x2 w2[4];
#pragma unroll
        for (int j = 0; j < 4; ++j) {
          f32x4 v = {t[4 * j], t[4 * j + 1], t[4 * j + 2], t[4 * j + 3]};
#pragma unroll
          for (int o = 0; o < 3; ++o) v += __builtin_bit_cast(f32x4, pv[o][j]);
          w2[j] = u32x2{cvtpk(v[0], v[1]), cvtpk(v[2], v[3])};
        }
        const unsigned obase = (unsigned)(((b * SQ + 32 * qb + (lane & 31)) * D + 160 * hl + 32 * nt + 8 * hi) * 2);
#pragma unroll
        for (int j = 0; j < 4; j += 2) {
          auto r0 = __builtin_amdgcn_permlane32_swap(w2[j][0], w2[j + 1][0], false, false);
          auto r1 = __builtin_amdgcn_permlane32_swap(w2[j][1], w2[j + 1][1], false, false);
          u32x4 w4 = {r0[0], r1[0], r0[1], r1[1]};
          __builtin_amdgcn_raw_buffer_store_b128(w4, sout, obase + 16 * j, 0, OUT_AUX);
        }
      }
    }
  }
}

extern "C" void kernel_launch(void* const* d_in, const int* in_sizes, int n_in, void* d_out, int out_size, void* d_ws, size_t ws_size, hipStream_t stream) {
  (void)hipFuncSetAttribute((const void*)mha_fused, hipFuncAttributeMaxDynamicSharedMemorySize, LDS_BYTES);
  mha_fused<<<256, 512, LDS_BYTES, stream>>>((const char*)d_in[0], (const char*)d_in[1], (const char*)d_in[2], (const char*)d_in[3], (const char*)d_in[4],
                                              (unsigned short*)d_out, (char*)d_ws);
}
```
